```python
import torch

def _softmax(x, axis=-1):
    return torch.softmax(x, dim=axis)
def _gelu(x):
    return torch.nn.functional.gelu(x, approximate="tanh")
def _silu(x):
    return torch.nn.functional.silu(x)
def _sigmoid(x):
    return torch.sigmoid(x)
def _layernorm(x, axis=-1, eps=1e-5):
    m = x.mean(dim=axis, keepdim=True)
    v = x.var(dim=axis, keepdim=True, unbiased=False)
    return (x - m) / torch.sqrt(v + eps)
def _scatter_add(tgt, idx, src, axis=0):
    return tgt.clone().index_add_(axis, idx, src)
def _segment_sum(src, seg, n):
    out = torch.zeros((n,) + src.shape[1:], dtype=src.dtype)
    return out.index_add_(0, seg, src)
def _pad(x, pw):
    flat = tuple(v for pair in reversed(pw) for v in pair)
    return torch.nn.functional.pad(x, flat)

kernel_name = 'parallel_sum__linear_kernel_hedgehog__moe_arch_mod__prompt'

def setup_inputs():

    _w = (lambda a: (lambda t: t.to(torch.bfloat16) if t.is_floating_point() else t)(torch.as_tensor(a)))
    def _A_setup():
        B, S, D, H = 1, 2048, 640, 32
        Dh = D // H
        return {
            "x": _w((lambda *s: torch.randn(*s, dtype=torch.bfloat16) * 0.05)(B, S, D)),
            "wqkv": _w((lambda *s: torch.randn(*s, dtype=torch.bfloat16) * 0.4)(D, 3 * D)),
            "w_phi": _w((lambda *s: torch.randn(*s, dtype=torch.bfloat16) * 0.4)(H, Dh, Dh) * (Dh ** -0.5)),
            "wo": _w((lambda *s: torch.randn(*s, dtype=torch.bfloat16) * 0.1)(D, D)),
        }
    _A_in = _A_setup()

    def _B_setup():
        B, S, D, F, C = 1, 2048, 640, 1536, 1
        return {
            "x": _w((lambda *s: torch.randn(*s, dtype=torch.bfloat16) * 0.05)(B, S, D)),
            "w_r": _w((lambda *s: torch.randn(*s, dtype=torch.bfloat16) * 0.05)(D)),
            "w_gate": _w((lambda *s: torch.randn(*s, dtype=torch.bfloat16) * 0.05)(D, F)),
            "w_up": _w((lambda *s: torch.randn(*s, dtype=torch.bfloat16) * 0.05)(D, F)),
            "w_down": _w((lambda *s: torch.randn(*s, dtype=torch.bfloat16) * 0.05)(F, D)),
        }
    _B_in = _B_setup()

    return {
        "A_x": _A_in["x"],
        "A_wqkv": _A_in["wqkv"],
        "A_w_phi": _A_in["w_phi"],
        "A_wo": _A_in["wo"],
        "B_x": _B_in["x"],
        "B_w_r": _B_in["w_r"],
        "B_w_gate": _B_in["w_gate"],
        "B_w_up": _B_in["w_up"],
        "B_w_down": _B_in["w_down"],
    }

def reference(A_x, A_wqkv, A_w_phi, A_wo, B_x, B_w_r, B_w_gate, B_w_up, B_w_down):
    def _A_ref(x, wqkv, w_phi, wo):
        B, S, D = x.shape
        H = 32; Dh = D // H
        qkv = (lambda x, *d: x.permute(*d))((x @ wqkv).reshape(B, S, 3, H, Dh), 2, 0, 3, 1, 4)
        q, k, v = qkv[0], qkv[1], qkv[2]
        pq = torch.einsum("bhsd,hdf->bhsf", q, w_phi)
        pk = torch.einsum("bhsd,hdf->bhsf", k, w_phi)
        phi_q = torch.concatenate([_softmax(pq, axis=-1), _softmax(-pq, axis=-1)], axis=-1)
        phi_k = torch.concatenate([_softmax(pk, axis=-1), _softmax(-pk, axis=-1)], axis=-1)
        kv = torch.einsum("bhsf,bhsd->bhfd", phi_k, v)
        z  = torch.einsum("bhsf->bhf", phi_k)
        num = torch.einsum("bhqf,bhfd->bhqd", phi_q, kv)
        den = torch.einsum("bhqf,bhf->bhq", phi_q, z)[..., None] + 1e-6
        o = num / den
        return (lambda x, *d: x.permute(*d))(o, 0, 2, 1, 3).reshape(B, S, D) @ wo

    def _B_ref(x, w_r, w_gate, w_up, w_down):
        B, S, D = x.shape
        C = 1
        r = x @ w_r
        idx = torch.argsort(-r, axis=-1)[:, :C]
        idx_e = (lambda x, n, axis: torch.repeat_interleave(x, n, dim=axis))(idx.reshape(B, C, 1), D, axis=2)
        x_sel = (lambda x, i, axis: torch.take_along_dim(x, i, dim=axis))(x, idx_e, axis=1)
        g = _silu(x_sel @ w_gate)
        u = x_sel @ w_up
        y = (g * u) @ w_down
        r_sel = _sigmoid((lambda x, i, axis: torch.take_along_dim(x, i, dim=axis))(r, idx, axis=1))
        y = y * r_sel.reshape(B, C, 1)
        b_off = (torch.arange(B) * S).reshape(B, 1)
        flat_idx = (idx + b_off).reshape(B * C)
        zero = (x * 0.0).reshape(B * S, D)
        upd = _scatter_add(zero, flat_idx, y.reshape(B * C, D), axis=0)
        return x + upd.reshape(B, S, D)

    x = A_x
    xn = _layernorm(x, axis=-1)
    return x + _A_ref(xn, A_wqkv, A_w_phi, A_wo) + _B_ref(xn, B_w_r, B_w_gate, B_w_up, B_w_down)
```

```cpp
#include <hip/hip_runtime.h>
#include <hip/hip_bf16.h>
typedef unsigned short u16;
__device__ __forceinline__ float bf2f(u16 v){ return __uint_as_float(((unsigned)v)<<16); }
__device__ __forceinline__ u16 f2bf(float f){ __hip_bfloat16 b = __float2bfloat16(f); return *reinterpret_cast<u16*>(&b); }
constexpr int S=2048, D=640, H=32, DH=20, F=1536;

__global__ void k_ln(const u16* x, const u16* wr, float* xn, float* r){
  int s = blockIdx.x; int t = threadIdx.x;
  float v[10]; float sum=0;
  for(int i=0;i<10;i++){ v[i]=bf2f(x[s*D+t+64*i]); sum+=v[i]; }
  for(int m=32;m>=1;m>>=1) sum+=__shfl_xor(sum,m);
  float mean=sum/D; float q=0;
  for(int i=0;i<10;i++){ float d=v[i]-mean; q+=d*d; }
  for(int m=32;m>=1;m>>=1) q+=__shfl_xor(q,m);
  float rs = 1.0f/sqrtf(q/D+1e-5f);
  float rr=0;
  for(int i=0;i<10;i++){ float n=bf2f(f2bf((v[i]-mean)*rs)); xn[s*D+t+64*i]=n; rr+=n*bf2f(wr[t+64*i]); }
  for(int m=32;m>=1;m>>=1) rr+=__shfl_xor(rr,m);
  if(t==0) r[s]=rr;
}
__global__ void k_gemm(const float* A, const u16* B, float* C, int M,int N,int K){
  int n = blockIdx.x*64+threadIdx.x; int m0 = blockIdx.y*8;
  float acc[8]={0,0,0,0,0,0,0,0};
  for(int k=0;k<K;k++){ float b=bf2f(B[k*N+n]);
    for(int i=0;i<8;i++) acc[i]+=A[(m0+i)*K+k]*b; }
  for(int i=0;i<8;i++) C[(m0+i)*N+n]=acc[i];
}
__global__ void k_feat(const float* qkv, const u16* wphi, float* phi){
  int id = blockIdx.x*64+threadIdx.x;
  int s=id>>6, wh=id&63, h=wh&31;
  float q[20], p[20];
  for(int d=0;d<20;d++) q[d]=qkv[s*1920+wh*20+d];
  float mx=-1e30f, mn=1e30f;
  for(int f=0;f<20;f++){ float a=0; for(int d=0;d<20;d++) a+=q[d]*bf2f(wphi[(h*20+d)*20+f]); p[f]=a; mx=fmaxf(mx,a); mn=fminf(mn,a);}
  float s1=0,s2=0;
  for(int f=0;f<20;f++){ s1+=expf(p[f]-mx); s2+=expf(mn-p[f]); }
  for(int f=0;f<20;f++){ phi[(size_t)id*40+f]=expf(p[f]-mx)/s1; phi[(size_t)id*40+20+f]=expf(mn-p[f])/s2; }
}
__global__ void k_kv(const float* qkv, const float* phi, float* kv){
  int h=blockIdx.x, f=blockIdx.y; int t=threadIdx.x;
  float acc[21]; for(int d=0;d<21;d++) acc[d]=0;
  for(int s=t;s<S;s+=64){ float p=phi[((size_t)s*64+32+h)*40+f];
    for(int d=0;d<20;d++) acc[d]+=p*qkv[s*1920+1280+h*20+d]; acc[20]+=p; }
  for(int d=0;d<21;d++){ float a=acc[d]; for(int m=32;m>=1;m>>=1) a+=__shfl_xor(a,m); if(t==0) kv[(h*40+f)*21+d]=a; }
}
__global__ void k_o(const float* phi, const float* kv, float* o){
  int id=blockIdx.x*64+threadIdx.x; int s=id>>5,h=id&31;
  float acc[21]; for(int d=0;d<21;d++) acc[d]=0;
  for(int f=0;f<40;f++){ float p=phi[((size_t)s*64+h)*40+f]; for(int d=0;d<21;d++) acc[d]+=p*kv[(h*40+f)*21+d]; }
  float den=acc[20]+1e-6f;
  for(int d=0;d<20;d++) o[s*D+h*20+d]=acc[d]/den;
}
__global__ void k_argmax(const float* r, int* idx){
  __shared__ float sv[256]; __shared__ int si[256];
  int t=threadIdx.x; float bv=-INFINITY; int bi=0;
  for(int s=t;s<S;s+=256){ if(r[s]>bv){bv=r[s];bi=s;} }
  sv[t]=bv; si[t]=bi; __syncthreads();
  if(t==0){ for(int i=1;i<256;i++) if(sv[i]>bv || (sv[i]==bv && si[i]<bi)){bv=sv[i];bi=si[i];} idx[0]=bi; }
}
__global__ void k_gu(const float* xn, const int* idx, const u16* wg, const u16* wu, float* hbuf){
  int f=blockIdx.x*64+threadIdx.x; const float* xs=xn+idx[0]*D;
  float g=0,u=0; for(int k=0;k<D;k++){ g+=xs[k]*bf2f(wg[k*F+f]); u+=xs[k]*bf2f(wu[k*F+f]); }
  hbuf[f]=g/(1.f+expf(-g))*u;
}
__global__ void k_down(const float* hbuf, const u16* wd, const float* r, const int* idx, float* y){
  int n=blockIdx.x*64+threadIdx.x; float a=0; for(int f=0;f<F;f++) a+=hbuf[f]*bf2f(wd[f*D+n]);
  float rs=1.f/(1.f+expf(-r[idx[0]])); y[n]=a*rs;
}
__global__ void k_final(const u16* x, const float* xn, const float* oa, const float* y, const int* idx, u16* out){
  int i=blockIdx.x*256+threadIdx.x; int s=i/D, n=i%D;
  float v=bf2f(x[i])+oa[i]+xn[i]; if(s==idx[0]) v+=y[n];
  out[i]=f2bf(v);
}
extern "C" void kernel_launch(void* const* d_in, const int* in_sizes, int n_in,
                              void* d_out, int out_size, void* d_ws, size_t ws_size,
                              hipStream_t stream) {
  const u16* x=(const u16*)d_in[0]; const u16* wqkv=(const u16*)d_in[1]; const u16* wphi=(const u16*)d_in[2]; const u16* wo=(const u16*)d_in[3];
  const u16* wr=(const u16*)d_in[5]; const u16* wg=(const u16*)d_in[6]; const u16* wu=(const u16*)d_in[7]; const u16* wd=(const u16*)d_in[8];
  float* ws=(float*)d_ws;
  float* xn=ws; float* r=xn+S*D; float* qkv=r+S; float* phi=qkv+S*1920; float* kv=phi+(size_t)S*64*40; float* o=kv+32*40*21; float* oa=o+S*D; float* hb=oa+S*D; float* y=hb+F; int* idx=(int*)(y+D);
  k_ln<<<S,64,0,stream>>>(x,wr,xn,r);
  k_gemm<<<dim3(1920/64,S/8),64,0,stream>>>(xn,wqkv,qkv,S,1920,D);
  k_feat<<<S,64,0,stream>>>(qkv,wphi,phi);
  k_kv<<<dim3(32,40),64,0,stream>>>(qkv,phi,kv);
  k_o<<<S*32/64,64,0,stream>>>(phi,kv,o);
  k_gemm<<<dim3(640/64,S/8),64,0,stream>>>(o,wo,oa,S,640,D);
  k_argmax<<<1,256,0,stream>>>(r,idx);
  k_gu<<<F/64,64,0,stream>>>(xn,idx,wg,wu,hb);
  k_down<<<D/64,64,0,stream>>>(hb,wd,r,idx,y);
  k_final<<<S*D/256,256,0,stream>>>(x,xn,oa,y,idx,(u16*)d_out);
}
```

```cpp
#include <hip/hip_runtime.h>
typedef unsigned short u16;
typedef unsigned int u32;
typedef unsigned long long u64;
typedef short s16x4 __attribute__((ext_vector_type(4)));
typedef __bf16 bf16x2 __attribute__((ext_vector_type(2)));
typedef __bf16 bf16x8 __attribute__((ext_vector_type(8)));
typedef float f32x2 __attribute__((ext_vector_type(2)));
typedef float f32x4 __attribute__((ext_vector_type(4)));
typedef u32 u32x2 __attribute__((ext_vector_type(2)));
typedef u32 u32x4 __attribute__((ext_vector_type(4)));
#define LAS __attribute__((address_space(3)))
#define GAS __attribute__((address_space(1)))
#define DI __device__ __forceinline__

namespace fk {
constexpr u32 WS_EPOCH = 0;
constexpr u32 WS_FLAG = 4096;
constexpr u32 WS_TMO = 8192;
constexpr u32 WS_CAND = 16384;
constexpr u32 WS_GU = 131072;
constexpr u32 WS_YP = 393216;
constexpr u32 WS_PART = 524288;
constexpr u32 PSTR = 3584;
constexpr u32 WS_O = WS_PART + 1048576;
constexpr u32 WS_END = WS_O + 2621440;
constexpr u32 SLOT = 32768, QO = 10240, XO = 16384;
constexpr u32 L_EX = 131072, L_MISC = 136192, L_TOTAL = 163840;
constexpr u32 L_CS = L_MISC;
constexpr u32 L_ZERO = L_MISC + 1024;
constexpr u32 L_CNT = L_MISC + 1088;
constexpr u32 L_WT = L_MISC + 1152;
constexpr u32 L_STAT = L_MISC + 4992;
constexpr u32 L_Y = L_MISC + 6016;
constexpr u32 L_SWR = L_MISC + 6400;
constexpr u32 L_SQ = L_MISC + 6432;
constexpr u32 L_KVR = 143360;
constexpr u32 L_HO = 147456;
constexpr u32 L_PK = 102400, L_V = 122880;
constexpr u32 L_RB = 102400;
constexpr u32 SENT = 0xFFFFFFFFu;
constexpr u32 OOB = 0x7FFF0000u;
constexpr u32 SPIN_CAP = 1u << 17;
constexpr u32 ONES = 0x3F803F80u;

DI float bflo(u32 v) { return __uint_as_float(v << 16); }
DI float bfhi(u32 v) { return __uint_as_float(v & 0xffff0000u); }
DI u32 pk2(float a, float b) { f32x2 f = {a, b}; return __builtin_bit_cast(u32, __builtin_convertvector(f, bf16x2)); }
DI float dot2(u32 a, u32 b, float c) { return __builtin_amdgcn_fdot2_f32_bf16(__builtin_bit_cast(bf16x2, a), __builtin_bit_cast(bf16x2, b), c, false); }
DI f32x4 mfma(u32x4 a, u32x4 b, f32x4 c) { return __builtin_amdgcn_mfma_f32_16x16x32_bf16(__builtin_bit_cast(bf16x8, a), __builtin_bit_cast(bf16x8, b), c, 0, 0, 0); }
DI u32x2 trd(LAS char* p) { return __builtin_bit_cast(u32x2, __builtin_amdgcn_ds_read_tr16_b64_v4i16((LAS s16x4*)p)); }
DI u32x4 trd2(LAS char* p, int d) { u32x2 a = trd(p), b = trd(p + d); return (u32x4){a.x, a.y, b.x, b.y}; }
DI float xs16(float v) { auto r = __builtin_amdgcn_permlane16_swap(__float_as_uint(v), __float_as_uint(v), false, false); return __uint_as_float(r[0]) + __uint_as_float(r[1]); }
DI float xs32(float v) { auto r = __builtin_amdgcn_permlane32_swap(__float_as_uint(v), __float_as_uint(v), false, false); return __uint_as_float(r[0]) + __uint_as_float(r[1]); }
DI float xmax16(float v) { auto r = __builtin_amdgcn_permlane16_swap(__float_as_uint(v), __float_as_uint(v), false, false); return fmaxf(__uint_as_float(r[0]), __uint_as_float(r[1])); }
DI float xmin16(float v) { auto r = __builtin_amdgcn_permlane16_swap(__float_as_uint(v), __float_as_uint(v), false, false); return fminf(__uint_as_float(r[0]), __uint_as_float(r[1])); }
template <int CTRL> DI u32 dpp(u32 v) { return __builtin_amdgcn_update_dpp(v, v, CTRL, 0xF, 0xF, false); }
DI u32 umax(u32 a, u32 b) { return a > b ? a : b; }
DI u32 rowmax_u32(u32 k) { k = umax(k, dpp<0x128>(k)); k = umax(k, dpp<0x124>(k)); k = umax(k, dpp<0x122>(k)); k = umax(k, dpp<0x121>(k)); return k; }
DI float xs8(float v) { return v + __uint_as_float(dpp<0x128>(__float_as_uint(v))); }
DI u32 san(u32 v) { return v < 0xFFFFFFFEu ? v : 0xFFFFFFFEu; }
DI u32x4 san4(f32x4 v) { return (u32x4){san(__float_as_uint(v[0])), san(__float_as_uint(v[1])), san(__float_as_uint(v[2])), san(__float_as_uint(v[3]))}; }
DI bool ok4(u32x4 v) { return v.x != SENT && v.y != SENT && v.z != SENT && v.w != SENT; }
DI void dma16(u32 voff, const void* sbase, u32 lds_dst) {
  asm volatile("s_mov_b32 m0, %2\n\ts_nop 0\n\tglobal_load_lds_dwordx4 %0, %1" :: "v"(voff), "s"(sbase), "s"(lds_dst) : "memory");
}
DI void dma16m(u32 voff, const void* sbase, u32 lds_dst, u64 mask) {
  asm volatile("s_mov_b32 m0, %2\n\ts_mov_b64 exec, %3\n\tglobal_load_lds_dwordx4 %0, %1\n\ts_mov_b64 exec, -1" :: "v"(voff), "s"(sbase), "s"(lds_dst), "s"(mask) : "memory");
}
#define STAMP(n) do {} while (0)
#define SB() __builtin_amdgcn_sched_barrier(0)
#define SGB(m, n) __builtin_amdgcn_sched_group_barrier(m, n, 0)
#define PIN(x) asm volatile("" : "+v"(x))
#define BARRIER_LDS() asm volatile("s_waitcnt lgkmcnt(0)\n\ts_barrier" ::: "memory")
#define WAITB(N) asm volatile("s_waitcnt vmcnt(" #N ") lgkmcnt(0)\n\ts_barrier" ::: "memory")

DI void cnt_signal(int idx) {
  const u32 a = L_CNT + idx * 4, one = 1u;
  asm volatile("s_waitcnt lgkmcnt(0)\n\ts_mov_b64 exec, 1\n\tds_add_u32 %0, %1\n\ts_mov_b64 exec, -1" :: "v"(a), "v"(one) : "memory");
}
DI void cnt_wait(LAS char* sm, int idx, u32 target) {
  for (;;) {
    const u32 v = __builtin_amdgcn_readfirstlane(__hip_atomic_load((LAS u32*)(sm + L_CNT + idx * 4), __ATOMIC_RELAXED, __HIP_MEMORY_SCOPE_WORKGROUP));
    if (v >= target) break;
  }
  asm volatile("" ::: "memory");
}

DI void dual_softmax(const f32x4 (&p)[3], float rs, float os, u32 (&o)[10]) {
  const float sc = rs * 1.4426950408889634f;
  float mx = p[0][0], mn = p[0][0];
#pragma unroll
  for (int e = 1; e < 10; e++) { mx = fmaxf(mx, p[e >> 2][e & 3]); mn = fminf(mn, p[e >> 2][e & 3]); }
  mx = xmax16(mx) * sc; mn = xmin16(mn) * sc;
  float ep[10], en[10], sp = 0.f, sn = 0.f;
#pragma unroll
  for (int e = 0; e < 10; e++) {
    ep[e] = __builtin_amdgcn_exp2f(__builtin_fmaf(p[e >> 2][e & 3], sc, -mx)); en[e] = __builtin_amdgcn_exp2f(__builtin_fmaf(p[e >> 2][e & 3], -sc, mn));
    sp += ep[e]; sn += en[e];
  }
  sp = __builtin_amdgcn_rcpf(xs16(sp)) * os; sn = __builtin_amdgcn_rcpf(xs16(sn)) * os;
#pragma unroll
  for (int e = 0; e < 5; e++) { o[e] = pk2(ep[2 * e] * sp, ep[2 * e + 1] * sp); o[5 + e] = pk2(en[2 * e] * sn, en[2 * e + 1] * sn); }
}

struct Cand { u32 idx; float r, mean, rstd; };
DI Cand poll_cand(const __amdgpu_buffer_rsrc_t wsr, int rep, int l, bool& tmo) {
  u32x4 a, b; u32 spins = 0;
  for (;;) {
    asm volatile("" ::: "memory");
    a = __builtin_amdgcn_raw_buffer_load_b128(wsr, WS_CAND + rep * 2048 + l * 16, 0, 16);
    b = __builtin_amdgcn_raw_buffer_load_b128(wsr, WS_CAND + rep * 2048 + 1024 + l * 16, 0, 16);
    if (__all(ok4(a) && ok4(b))) break;
    if (++spins > SPIN_CAP) { tmo = true; break; }
    __builtin_amdgcn_s_sleep(2);
  }
  u32 mk = rowmax_u32(umax(a.x, b.x));
  { auto r = __builtin_amdgcn_permlane16_swap(mk, mk, false, false); mk = umax(r[0], r[1]); }
  { auto r = __builtin_amdgcn_permlane32_swap(mk, mk, false, false); mk = umax(r[0], r[1]); }
  const u64 ba = __ballot(a.x == mk), bbal = __ballot(b.x == mk);
  if (ba == 0) a = b;
  const int src = __builtin_ctzll((ba ? ba : bbal) | (1ull << 63));
  Cand c;
  c.idx = __builtin_amdgcn_readlane(a.y, src) & 2047u;
  c.r = __uint_as_float((mk & 0x80000000u) ? (mk & 0x7FFFFFFFu) : ~mk);
  c.mean = __uint_as_float(__builtin_amdgcn_readlane(a.z, src));
  c.rstd = __uint_as_float(__builtin_amdgcn_readlane(a.w, src));
  return c;
}
DI void check_flags(const __amdgpu_buffer_rsrc_t wsr, u32 tag, int l, bool& tmo) {
  u32 spins = 0;
  for (;;) {
    asm volatile("" ::: "memory");
    const u32x4 fl = __builtin_amdgcn_raw_buffer_load_b128(wsr, WS_FLAG + l * 16, 0, 16);
    if (__all(fl.x == tag && fl.y == tag && fl.z == tag && fl.w == tag)) break;
    if (++spins > SPIN_CAP) { tmo = true; break; }
    __builtin_amdgcn_s_sleep(1);
  }
}

__global__ __launch_bounds__(512, 2) void fused(const u16* __restrict__ x, const u16* __restrict__ wqkv, const u16* __restrict__ wphi,
                                                const u16* __restrict__ wo, const u16* __restrict__ wr, const u16* __restrict__ wg,
                                                const u16* __restrict__ wu, const u16* __restrict__ wd, u16* __restrict__ out, char* __restrict__ ws) {
  extern __shared__ __attribute__((aligned(16))) char smem_[];
  LAS char* sm = (LAS char*)smem_;
  STAMP(0);
  const int tid = threadIdx.x, l = tid & 63, c16 = l & 15, g = l >> 4;
  const int w = __builtin_amdgcn_readfirstlane(tid >> 6);
  const int v = w & 3;
  const int bid = blockIdx.x, xcd = bid & 7, slot = bid >> 3;
  const int bi = 4 * (xcd >> 1) + (slot & 3), bj = 8 * (xcd & 1) + (slot >> 2);
  const int r0 = (((slot >> 2) * 5 / 2 + (slot & 3) * 5) >> 1) % 10;
  const int qj = bj ^ 1, bq = bj >> 1;
  const int q4 = c16 >> 2, p4 = c16 & 3;
  const int th = bj & 1, co = bj >> 1;
  const int bb = bi * 16 + bj;
  const int fs = bb % 24, ks = bb / 24;
  const bool bon = bb < 240;
  const int tile = 2 * v + (w >> 2);
  const __amdgpu_buffer_rsrc_t wsr = __builtin_amdgcn_make_buffer_rsrc(ws, 0, WS_END, 0x00020000);
  bool tmo = false;
  LAS char* kvrd = sm + (4 * g + q4) * 160 + p4 * 40;
  LAS char* qrd = sm + QO + (4 * g + q4) * 96 + p4 * 8;
  LAS char* xrd0 = sm + XO + v * 4096 + (c16 >> 3) * 1024 + (c16 & 7) * 128 + ((g ^ (c16 >> 1)) * 16);
  LAS char* xrd1 = sm + XO + v * 4096 + (c16 >> 3) * 1024 + (c16 & 7) * 128 + (((4 + g) ^ (c16 >> 1)) * 16);
  u32 tag = 0;
  u32 phq[10];
  u32x4 a2k[3][3];
  u32 a2ad[3];
  auto addr_a2k = [&]() {
    const int hr = q4 >> 1;
#pragma unroll
    for (int t = 0; t < 3; t++) {
      const int fl_ = 4 * t + p4; const bool fv = fl_ < 10 && g == hr; const int f = 10 * (q4 & 1) + fl_;
      a2ad[t] = fv ? L_WT + ((2 * (bj & 1) + hr) * 20 + f) * 48 : L_ZERO;
      PIN(a2ad[t]);
    }
  };
  auto load_a2k = [&]() {
#pragma unroll
    for (int t = 0; t < 3; t++)
#pragma unroll
      for (int s2 = 0; s2 < 3; s2++) a2k[t][s2] = *(LAS u32x4*)(sm + a2ad[t] + 16 * s2);
  };
  auto phik_tile = [&](u32x4 b0, u32x4 b1, u32x4 b2, float rs) {
    f32x4 pk[3];
#pragma unroll
    for (int t = 0; t < 3; t++) pk[t] = mfma(a2k[t][0], b0, (f32x4){0.f, 0.f, 0.f, 0.f});
#pragma unroll
    for (int t = 0; t < 3; t++) pk[t] = mfma(a2k[t][1], b1, pk[t]);
#pragma unroll
    for (int t = 0; t < 3; t++) pk[t] = mfma(a2k[t][2], b2, pk[t]);
    u32 ph[10];
    dual_softmax(pk, rs, rs, ph);
    LAS u32x2* pp = (LAS u32x2*)(sm + L_PK + (tile * 16 + c16) * 160 + g * 40);
#pragma unroll
    for (int e = 0; e < 5; e++) pp[e] = (u32x2){ph[2 * e], ph[2 * e + 1]};
  };
  auto phiq_tile = [&](u32x4 b0, u32x2 qa, float rs) {
    u32x4 a0[3]; u32x2 a1[3];
    const int hr = q4 >> 1;
#pragma unroll
    for (int t = 0; t < 3; t++) {
      const int fl_ = 4 * t + p4; const bool fv = fl_ < 10; const int f = 10 * (q4 & 1) + fl_;
      const u32 rowb = L_WT + ((2 * (qj & 1) + hr) * 20 + f) * 48;
      const u32 alo = (fv && hr == 0) ? rowb + 8 * g : L_ZERO;
      const u32 ahi = (fv && ((g == 0) == (hr == 0))) ? rowb + (g == 0 ? 32 : 8 * (g - 1)) : L_ZERO;
      const u32 aa = (fv && hr == 1 && g < 2) ? rowb + 24 + 8 * g : L_ZERO;
      const u32x2 lo = *(LAS u32x2*)(sm + alo), hi = *(LAS u32x2*)(sm + ahi);
      a0[t] = (u32x4){lo.x, lo.y, hi.x, hi.y}; a1[t] = *(LAS u32x2*)(sm + aa);
    }
    SB();
    f32x4 pq[3];
#pragma unroll
    for (int t = 0; t < 3; t++) pq[t] = mfma(a0[t], b0, (f32x4){0.f, 0.f, 0.f, 0.f});
#pragma unroll
    for (int t = 0; t < 3; t++) pq[t] = mfma((u32x4){a1[t].x, a1[t].y, 0u, 0u}, (u32x4){qa.x, qa.y, 0u, 0u}, pq[t]);
    dual_softmax(pq, rs, 1.f, phq);
  };

  if (w >= 4) {
    __builtin_amdgcn_s_setprio(3);
    const int r8 = l >> 3;
    const u32 xoff = (bi * 128 + v * 32 + r8) * 1280 + (((l & 7) ^ (r8 >> 1)) * 16);
    auto rotd = [&](int c) { int d = c + r0; return d >= 10 ? d - 10 : d; };
    auto issue_x = [&](int c) {
      const char* b = (const char*)x + rotd(c) * 128;
      const u32 dst = (c & 3) * SLOT + XO + v * 4096;
      dma16(xoff, b, dst); dma16((xoff ^ 64u) + 8 * 1280, b, dst + 1024);
      dma16(xoff + 16 * 1280, b, dst + 2048); dma16((xoff ^ 64u) + 24 * 1280, b, dst + 3072);
    };
    issue_x(0);
    u32 wvoff[3], wooff[3], wqoff[2]; u64 qm[2];
#pragma unroll
    for (int u = 0; u < 3; u++) {
      const int c = 64 * u + (u == 2 ? (l & 31) : l), rho = 16 * v + c / 10, cc = c % 10;
      const int k = (rho & 32) + 8 * ((rho >> 2) & 3) + 4 * ((rho >> 4) & 1) + (rho & 3);
      wvoff[u] = k * 3840 + (cc < 5 ? 1280 + 16 * cc : 2560 + 16 * (cc - 5)) + 80 * bj;
      wooff[u] = k * 1280 + co * 160 + 16 * cc;
    }
#pragma unroll
    for (int u = 0; u < 2; u++) {
      const int c = 64 * u + (u == 1 ? (l & 31) : l), rho = 16 * v + c / 6, cc = c % 6;
      const int k = (rho & 32) + 8 * ((rho >> 2) & 3) + 4 * ((rho >> 4) & 1) + (rho & 3);
      wqoff[u] = k * 3840 + 80 * qj + 16 * cc;
      qm[u] = __ballot(cc < 5 && (u == 0 || l < 32));
    }
    auto issue_w = [&](int c) {
      const char* wb = (const char*)wqkv + rotd(c) * 245760;
      const u32 sb = (c & 3) * SLOT;
      dma16(wvoff[0], wb, sb + v * 2560); dma16(wvoff[1], wb, sb + v * 2560 + 1024); dma16m(wvoff[2], wb, sb + v * 2560 + 2048, 0xFFFFFFFFull);
      dma16m(wqoff[0], wb, sb + QO + v * 1536, qm[0]); dma16m(wqoff[1], wb, sb + QO + v * 1536 + 1024, qm[1]);
    };
    auto issue_wo = [&](int s) {
      const char* wb = (const char*)wo + s * 81920;
      dma16(wooff[0], wb, s * 10240 + v * 2560); dma16(wooff[1], wb, s * 10240 + v * 2560 + 1024); dma16m(wooff[2], wb, s * 10240 + v * 2560 + 2048, 0xFFFFFFFFull);
    };
    issue_w(0); issue_x(1); issue_w(1);
    STAMP(1);
    u32 ca[5];
    {
      const int ht = tid - 256;
      ca[0] = ht < 224 ? WS_PART + (bj * 16 + bi) * PSTR + ht * 16 : OOB;
      const u32 ob = WS_O + ((bi * 8 + 2 * v) * 80 + 5 * qj) * 256;
      ca[1] = ob + l * 16; ca[2] = ob + 20480 + l * 16;
      ca[3] = l < 32 ? ob + (l >> 4) * 20480 + 1024 + c16 * 16 : OOB;
      u32 a = OOB;
      if (ht < 8) a = WS_CAND + bj * 2048 + (bi * 8 + ht) * 16;
      else if (ht >= 64 && ht < 128 && bon) a = WS_GU + (fs * 20 + ks * 2) * 512 + (ht - 64) * 16;
      else if (ht >= 128 && ht < 144 && bon) a = WS_YP + fs * 2560 + ks * 256 + (ht - 128) * 16;
      ca[4] = a;
    }
    addr_a2k();
    f32x4 csa = {0.f, 0.f, 0.f, 0.f}, csb = {0.f, 0.f, 0.f, 0.f};
    float sq[2] = {0.f, 0.f};
    const u32x4 ones4 = {ONES, ONES, ONES, ONES};
    LAS char* csrd = kvrd + 8 * v;
    LAS char* cs2a = v == 0 ? kvrd + 32 : qrd + 32 * (v - 1);
    LAS char* cs2b = cs2a + (v == 0 ? 2560 : 1536);
    LAS char* cs2c = cs2a + (v == 0 ? 5120 : 3072);
    LAS char* cs2d = cs2b + (v == 0 ? 5120 : 3072);
#pragma unroll
    for (int c = 0; c < 10; c++) {
      if (c == 0) WAITB(9); else if (c <= 7) WAITB(18); else if (c == 8) WAITB(14); else WAITB(5);
      if (c == 0) STAMP(2);
      const u32 sb = (c & 3) * SLOT;
      if (c == 0) { issue_x(2); issue_w(2); }
      if (c <= 6) { issue_x(c + 3); issue_w(c + 3); }
      if (c == 7) {
        const u32x4 sv = {SENT, SENT, SENT, SENT};
#pragma unroll
        for (int n = 0; n < 5; n++) __builtin_amdgcn_raw_buffer_store_b128(sv, wsr, ca[n], 0, 16);
      }
      SB();
      u32x4 xv[4];
#pragma unroll
      for (int t = 0; t < 2; t++) { xv[2 * t] = *(LAS u32x4*)(xrd0 + sb + t * 2048); xv[2 * t + 1] = *(LAS u32x4*)(xrd1 + sb + t * 2048); }
      const u32x4 c0 = trd2(csrd + sb, 2560), c1 = trd2(csrd + sb + 5120, 2560);
      const u32x2 d0 = trd(cs2a + sb), d1 = trd(cs2b + sb), d2 = trd(cs2c + sb), d3 = trd(cs2d + sb);
      if (c == 8) load_a2k();
      SB();
      csa = mfma(c0, ones4, csa);
      csb = mfma((u32x4){d0.x, d0.y, d1.x, d1.y}, ones4, csb);
      csa = mfma(c1, ones4, csa);
      csb = mfma((u32x4){d2.x, d2.y, d3.x, d3.y}, ones4, csb);
#pragma unroll
      for (int t = 0; t < 2; t++)
#pragma unroll
        for (int m = 0; m < 4; m++) { sq[t] = dot2(xv[2 * t][m], xv[2 * t][m], sq[t]); sq[t] = dot2(xv[2 * t + 1][m], xv[2 * t + 1][m], sq[t]); }
      SB();
    }
    if (c16 == 0) { *(LAS f32x4*)(sm + L_CS + v * 64 + g * 16) = csa; *(LAS f32x4*)(sm + L_CS + (4 + v) * 64 + g * 16) = csb; }
    sq[0] = xs32(xs16(sq[0])); sq[1] = xs32(xs16(sq[1]));
    if (g < 2) *(LAS float*)(sm + L_SQ + ((2 * v + g) * 16 + c16) * 4) = g == 0 ? sq[0] : sq[1];
    BARRIER_LDS();
    STAMP(3);
    __builtin_amdgcn_s_setprio(0);
    tag = __builtin_amdgcn_readfirstlane(*(LAS u32*)(sm + L_SWR + 16));
    cnt_wait(sm, 8 + v, 1);
    float rs1;
    {
      const u32x4 b0 = *(LAS u32x4*)(sm + L_HO + v * 4096 + l * 16), b1 = *(LAS u32x4*)(sm + L_HO + v * 4096 + 1024 + l * 16);
      const u32x4 b2 = *(LAS u32x4*)(sm + L_HO + v * 4096 + 2048 + l * 16);
      rs1 = __uint_as_float(b2.z);
      phik_tile(b0, b1, (u32x4){b2.x, b2.y, 0u, 0u}, rs1);
    }
    asm volatile("s_waitcnt vmcnt(0)" ::: "memory");
    cnt_signal(0);
    STAMP(4);
    if (v == 0) { cnt_wait(sm, 0, 8); if (l == 0) __hip_atomic_store((GAS u32*)(ws + WS_FLAG) + bid, tag, __ATOMIC_RELAXED, __HIP_MEMORY_SCOPE_AGENT); }
    u32x4 bw[8];
    if (bon) {
      if (v == 1 || v == 2) {
        const u32 o = ((ks * 64 + (v - 1) * 32 + (l >> 3)) * 1536 + fs * 64 + (l & 7) * 8);
#pragma unroll
        for (int n = 0; n < 4; n++) { bw[n] = *(const u32x4*)(wg + o + n * 8 * 1536); bw[4 + n] = *(const u32x4*)(wu + o + n * 8 * 1536); }
      } else if (v == 0) {
        const u32 o = ((fs * 64 + (l >> 3)) * 640 + ks * 64 + (l & 7) * 8);
#pragma unroll
        for (int n = 0; n < 8; n++) bw[n] = *(const u32x4*)(wd + o + n * 8 * 640);
      }
    }
#pragma unroll
    for (int s = 0; s < 10; s++) issue_wo(s);
    STAMP(5);
    cnt_wait(sm, 12 + v, 1);
    phiq_tile(*(LAS u32x4*)(sm + L_HO + v * 4096 + l * 16), *(LAS u32x2*)(sm + L_HO + v * 4096 + 1024 + l * 8), rs1);
    STAMP(6);
    cnt_wait(sm, 2, 1);
    STAMP(7);
    if ((v == 1 || v == 2) && bon) {
      const Cand cd = poll_cand(wsr, bj, l, tmo);
      float ag[8], au[8];
#pragma unroll
      for (int e = 0; e < 8; e++) { ag[e] = 0.f; au[e] = 0.f; }
      const u16* xr = x + cd.idx * 640 + ks * 64 + (v - 1) * 32 + (l >> 3);
#pragma unroll
      for (int n = 0; n < 4; n++) {
        const float xs = (bflo(xr[n * 8]) - cd.mean) * cd.rstd;
#pragma unroll
        for (int e = 0; e < 4; e++) {
          ag[2 * e] += xs * bflo(bw[n][e]); ag[2 * e + 1] += xs * bfhi(bw[n][e]);
          au[2 * e] += xs * bflo(bw[4 + n][e]); au[2 * e + 1] += xs * bfhi(bw[4 + n][e]);
        }
      }
#pragma unroll
      for (int e = 0; e < 8; e++) { ag[e] = xs32(xs16(xs8(ag[e]))); au[e] = xs32(xs16(xs8(au[e]))); }
      const int sel = l >> 3;
      if (sel < 4) {
        f32x4 vv;
        if (sel == 0) vv = (f32x4){ag[0], ag[1], ag[2], ag[3]}; else if (sel == 1) vv = (f32x4){ag[4], ag[5], ag[6], ag[7]};
        else if (sel == 2) vv = (f32x4){au[0], au[1], au[2], au[3]}; else vv = (f32x4){au[4], au[5], au[6], au[7]};
        __builtin_amdgcn_raw_buffer_store_b128(san4(vv), wsr, WS_GU + (fs * 20 + ks * 2 + v - 1) * 512 + (sel >> 1) * 256 + (l & 7) * 32 + (sel & 1) * 16, 0, 16);
      }
    }
    STAMP(8);
    asm volatile("s_waitcnt vmcnt(0)" ::: "memory");
    BARRIER_LDS();
    STAMP(10);
#define O_STAGE() do { \
    u32x4 A[3][3]; \
    _Pragma("unroll") for (int u = 0; u < 3; u++) { \
      int row; \
      if (u < 2) { const int c = 8 * q4 + 4 * u + p4; row = c + (c >= 20); } \
      else row = p4 < 2 ? 33 + 2 * q4 + p4 : (p4 == 2 ? 20 : 41); \
      const int hrow = row >= 21; \
      const u32 ad = hrow == (g >> 1) ? L_KVR + row * 96 + (g & 1) * 48 : L_ZERO; \
      _Pragma("unroll") for (int s3 = 0; s3 < 3; s3++) A[u][s3] = *(LAS u32x4*)(sm + ad + 16 * s3); \
    } \
    SB(); \
    f32x4 od[3]; \
    _Pragma("unroll") for (int u = 0; u < 3; u++) od[u] = mfma(A[u][0], (u32x4){phq[0], phq[1], phq[2], phq[3]}, (f32x4){0.f, 0.f, 0.f, 0.f}); \
    _Pragma("unroll") for (int u = 0; u < 3; u++) od[u] = mfma(A[u][1], (u32x4){phq[4], phq[5], phq[6], phq[7]}, od[u]); \
    _Pragma("unroll") for (int u = 0; u < 3; u++) od[u] = mfma(A[u][2], (u32x4){phq[8], phq[9], 0u, 0u}, od[u]); \
    const float ia = __builtin_amdgcn_rcpf(od[2][2] + 1e-6f), ib = __builtin_amdgcn_rcpf(od[2][3] + 1e-6f); \
    const float i0 = g <= 2 ? ia : ib, i1 = g <= 1 ? ia : ib; \
    const u32 ob = WS_O + ((bi * 8 + tile) * 80 + 5 * qj) * 256; \
    __builtin_amdgcn_raw_buffer_store_b128((u32x4){san(pk2(od[0][0] * i0, od[0][1] * i0)), san(pk2(od[0][2] * i0, od[0][3] * i0)), san(pk2(od[1][0] * i1, od[1][1] * i1)), san(pk2(od[1][2] * i1, od[1][3] * i1))}, wsr, ob + l * 16, 0, 16); \
    __builtin_amdgcn_raw_buffer_store_b32(san(pk2(od[2][0] * ib, od[2][1] * ib)), wsr, ob + 1024 + c16 * 16 + 4 * g, 0, 16); \
  } while (0)
    O_STAGE();
    STAMP(11);
    if (v == 0 && bon) {
      const u32 base = WS_GU + fs * 20 * 512 + l * 4;
      u32 vg[20], vu[20]; u32 spins = 0;
      for (;;) {
        asm volatile("" ::: "memory");
        bool ok = true;
#pragma unroll
        for (int p = 0; p < 20; p++) { vg[p] = __builtin_amdgcn_raw_buffer_load_b32(wsr, base, p * 512, 16); vu[p] = __builtin_amdgcn_raw_buffer_load_b32(wsr, base + 256, p * 512, 16); ok &= vg[p] != SENT && vu[p] != SENT; }
        if (__all(ok)) break;
        if (++spins > SPIN_CAP) { tmo = true; break; }
        __builtin_amdgcn_s_sleep(2);
      }
      float gg = 0.f, uu = 0.f;
#pragma unroll
      for (int p = 0; p < 20; p++) { gg += __uint_as_float(vg[p]); uu += __uint_as_float(vu[p]); }
      const float hv = gg * __builtin_amdgcn_rcpf(1.f + __expf(-gg)) * uu;
      float y[8];
#pragma unroll
      for (int e = 0; e < 8; e++) y[e] = 0.f;
#pragma unroll
      for (int n = 0; n < 8; n++) {
        const float hh = __shfl(hv, (l >> 3) + 8 * n);
#pragma unroll
        for (int e = 0; e < 4; e++) { y[2 * e] += hh * bflo(bw[n][e]); y[2 * e + 1] += hh * bfhi(bw[n][e]); }
      }
#pragma unroll
      for (int e = 0; e < 8; e++) y[e] = xs32(xs16(xs8(y[e])));
      if (l < 16) {
        const f32x4 v4 = l < 8 ? (f32x4){y[0], y[1], y[2], y[3]} : (f32x4){y[4], y[5], y[6], y[7]};
        __builtin_amdgcn_raw_buffer_store_b128(san4(v4), wsr, WS_YP + fs * 2560 + ks * 256 + (l & 7) * 32 + (l >> 3) * 16, 0, 16);
      }
    }
    if (v == 3) {
      const Cand cd = poll_cand(wsr, bj, l, tmo);
      const bool mine = (int)(cd.idx >> 6) == bi * 2 + th;
      if (mine) {
        const int grp = l / 20, c4 = l % 20;
        const u32 base = grp < 3 ? WS_YP + (grp * 8) * 2560 + (co * 80 + c4 * 4) * 4 : OOB;
        u32x4 vv[8]; u32 sp2 = 0;
        for (;;) {
          asm volatile("" ::: "memory");
          bool ok = true;
#pragma unroll
          for (int p = 0; p < 8; p++) { vv[p] = __builtin_amdgcn_raw_buffer_load_b128(wsr, base, p * 2560, 16); ok &= ok4(vv[p]); }
          if (__all(ok)) break;
          if (++sp2 > SPIN_CAP) { tmo = true; break; }
        }
        f32x4 a = __builtin_bit_cast(f32x4, vv[0]);
#pragma unroll
        for (int p = 1; p < 8; p++) a += __builtin_bit_cast(f32x4, vv[p]);
        f32x4 t;
#pragma unroll
        for (int e = 0; e < 4; e++) t[e] = a[e] + __shfl(a[e], c4 + 20) + __shfl(a[e], c4 + 40);
        const float sg = __builtin_amdgcn_rcpf(1.f + __expf(-cd.r));
        if (l < 20) *(LAS f32x4*)(sm + L_Y + l * 16) = t * sg;
      }
      if (l == 0) *(LAS u32*)(sm + L_Y + 320) = mine ? cd.idx : 0xFFFFu;
      cnt_signal(3);
      if (bid == 0 && l == 0) __hip_atomic_store((GAS u32*)(ws + WS_EPOCH), tag, __ATOMIC_RELAXED, __HIP_MEMORY_SCOPE_AGENT);
    }
    STAMP(12);
  } else {
    {
      const u32x4 wrc = *(const u32x4*)(wr + (tid < 80 ? tid : 0) * 8);
      const u32x4 wpc = *(const u32x4*)(wphi + 1600 * bq + (tid < 200 ? tid : 0) * 8);
      const u32 ep_raw = __hip_atomic_load((GAS u32*)(ws + WS_EPOCH), __ATOMIC_RELAXED, __HIP_MEMORY_SCOPE_AGENT);
      if (tid < 32) *(LAS u32*)(sm + L_ZERO + tid * 4) = 0u;
      *(LAS u32x4*)(sm + (tid >> 6) * SLOT + QO + (tid & 63) * 96 + 80) = (u32x4){0u, 0u, 0u, 0u};
      if (tid < 80) *(LAS u32x2*)(sm + L_WT + tid * 48 + 40) = (u32x2){0u, 0u};
      if (tid < 84) *(LAS u32x2*)(sm + L_KVR + tid * 48 + 40) = (u32x2){0u, 0u};
      float swp = 0.f;
      if (tid < 80) {
        const int k0 = tid * 8; int st = (k0 >> 6) - r0; st = st < 0 ? st + 10 : st;
        const int rb = st * 64 + (k0 & 32) + 4 * ((k0 >> 3) & 3);
#pragma unroll
        for (int j = 0; j < 8; j++) {
          const u32 hv = (j & 1) ? (wrc[j >> 1] & 0xFFFF0000u) : (wrc[j >> 1] << 16);
          *(LAS u32x2*)(sm + L_EX + (rb + 16 * (j >> 2) + (j & 3)) * 8) = (u32x2){0x3F80u | hv, 0u};
          swp += __uint_as_float(hv);
        }
      }
      if (tid < 200) {
#pragma unroll
        for (int j = 0; j < 8; j++) {
          const int e = tid * 8 + j, h = e / 400, r = e % 400, d = r / 20, f = r % 20;
          *(LAS u16*)(sm + L_WT + (h * 20 + f) * 48 + d * 2) = (u16)((j & 1) ? (wpc[j >> 1] >> 16) : (wpc[j >> 1] & 0xFFFFu));
        }
      }
      tag = __builtin_amdgcn_readfirstlane(ep_raw) + 1u;
      swp += __uint_as_float(dpp<0x128>(__float_as_uint(swp))); swp += __uint_as_float(dpp<0x124>(__float_as_uint(swp)));
      swp += __uint_as_float(dpp<0x122>(__float_as_uint(swp))); swp += __uint_as_float(dpp<0x121>(__float_as_uint(swp)));
      swp = xs32(xs16(swp));
      if (l == 0 && v < 2) *(LAS float*)(sm + L_SWR + v * 4) = swp;
      if (tid == 0) *(LAS u32*)(sm + L_SWR + 16) = tag;
    }
    addr_a2k();
    STAMP(1);
    f32x4 acc[2][9];
#pragma unroll
    for (int T = 0; T < 9; T++) { acc[0][T] = (f32x4){0.f, 0.f, 0.f, 0.f}; acc[1][T] = (f32x4){0.f, 0.f, 0.f, 0.f}; }
    LAS char* exrd = sm + L_EX + (4 * g + q4) * 8;
    u32x4 fb[9], xb[2];
#pragma unroll
    for (int c = 0; c < 10; c++) {
      BARRIER_LDS();
      if (c == 0) STAMP(2);
      const u32 sb = (c & 3) * SLOT;
      u32x4 fa[9], xa[2];
#pragma unroll
      for (int t = 0; t < 2; t++) xa[t] = *(LAS u32x4*)(xrd0 + sb + t * 2048);
#pragma unroll
      for (int T = 0; T < 5; T++) fa[T] = trd2(kvrd + sb + 8 * T, 2560);
      fa[5] = trd2(exrd + c * 512, 128);
#pragma unroll
      for (int T = 0; T < 3; T++) fa[6 + T] = trd2(qrd + sb + 32 * T, 1536);
      if (c > 0) {
#pragma unroll
        for (int T = 0; T < 9; T++) { acc[0][T] = mfma(fb[T], xb[0], acc[0][T]); acc[1][T] = mfma(fb[T], xb[1], acc[1][T]); }
#pragma unroll
        for (int i = 0; i < 2; i++) { SGB(0x8, 1); SGB(0x100, 2); }
#pragma unroll
        for (int i = 0; i < 16; i++) { SGB(0x8, 1); SGB(0x100, 1); }
      }
      SB();
#pragma unroll
      for (int t = 0; t < 2; t++) xb[t] = *(LAS u32x4*)(xrd1 + sb + t * 2048);
#pragma unroll
      for (int T = 0; T < 5; T++) fb[T] = trd2(kvrd + sb + 5120 + 8 * T, 2560);
      fb[5] = trd2(exrd + c * 512 + 256, 128);
#pragma unroll
      for (int T = 0; T < 3; T++) fb[6 + T] = trd2(qrd + sb + 3072 + 32 * T, 1536);
#pragma unroll
      for (int T = 0; T < 9; T++) { acc[0][T] = mfma(fa[T], xa[0], acc[0][T]); acc[1][T] = mfma(fa[T], xa[1], acc[1][T]); }
#pragma unroll
      for (int i = 0; i < 7; i++) { SGB(0x8, 1); SGB(0x100, 2); }
#pragma unroll
      for (int i = 0; i < 6; i++) { SGB(0x8, 1); SGB(0x100, 1); }
      SGB(0x8, 5);
      SB();
      if (c == 9) {
#pragma unroll
        for (int T = 0; T < 6; T++) { acc[1][T] = mfma(fb[T], xb[1], acc[1][T]); acc[0][T] = mfma(fb[T], xb[0], acc[0][T]); }
        SB();
      }
    }
    BARRIER_LDS();
    STAMP(3);
    float mean[2], rstd[2];
    {
      f32x4 cs[5];
#pragma unroll
      for (int T = 0; T < 5; T++) cs[T] = *(LAS f32x4*)(sm + L_CS + T * 64 + g * 16);
      const float s2a = *(LAS float*)(sm + L_SQ + ((2 * v) * 16 + c16) * 4), s2b = *(LAS float*)(sm + L_SQ + ((2 * v + 1) * 16 + c16) * 4);
      load_a2k();
#pragma unroll
      for (int T = 6; T < 9; T++) { acc[1][T] = mfma(fb[T], xb[1], acc[1][T]); acc[0][T] = mfma(fb[T], xb[0], acc[0][T]); }
      SB();
      u32 P[2][10];
      float sd[2];
#pragma unroll
      for (int t = 1; t >= 0; t--) {
        mean[t] = acc[t][5][0] * (1.f / 640.f);
        const float var = fmaxf((t ? s2b : s2a) * (1.f / 640.f) - mean[t] * mean[t], 0.f) + 1e-5f;
        rstd[t] = __builtin_amdgcn_rsqf(var);
        sd[t] = var * rstd[t];
#pragma unroll
        for (int T = 0; T < 5; T++) {
          const f32x4 vv = acc[t][T] - cs[T] * mean[t];
          P[t][2 * T] = pk2(vv[0], vv[1]); P[t][2 * T + 1] = pk2(vv[2], vv[3]);
        }
        if (t == 1) {
          *(LAS u32x4*)(sm + L_HO + v * 4096 + l * 16) = (u32x4){P[1][0], P[1][1], P[1][2], P[1][3]};
          *(LAS u32x4*)(sm + L_HO + v * 4096 + 1024 + l * 16) = (u32x4){P[1][4], P[1][5], P[1][6], P[1][7]};
          *(LAS u32x4*)(sm + L_HO + v * 4096 + 2048 + l * 16) = (u32x4){P[1][8], P[1][9], __float_as_uint(rstd[1]), 0u};
          cnt_signal(8 + v);
        }
      }
      phik_tile((u32x4){P[0][0], P[0][1], P[0][2], P[0][3]}, (u32x4){P[0][4], P[0][5], P[0][6], P[0][7]}, (u32x4){P[0][8], P[0][9], 0u, 0u}, rstd[0]);
      if (g >= 2) {
#pragma unroll
        for (int t = 0; t < 2; t++) {
          LAS u32x4* vp = (LAS u32x4*)(sm + L_V + ((2 * v + t) * 16 + c16) * 96 + (g - 2) * 48);
          vp[0] = (u32x4){P[t][0], P[t][1], P[t][2], P[t][3]}; vp[1] = (u32x4){P[t][4], P[t][5], P[t][6], P[t][7]}; vp[2] = (u32x4){P[t][8], P[t][9], pk2(sd[t], 0.f), 0u};
        }
      } else {
        *(LAS f32x2*)(sm + L_STAT + ((2 * v + g) * 16 + c16) * 8) = g == 0 ? (f32x2){mean[0], rstd[0]} : (f32x2){mean[1], rstd[1]};
      }
    }
    cnt_signal(0);
    STAMP(4);
    cnt_wait(sm, 0, 8);
    STAMP(5);
    {
      const int h = v >> 1, nt = v & 1;
      f32x4 kk[3];
#pragma unroll
      for (int m = 0; m < 3; m++) kk[m] = (f32x4){0.f, 0.f, 0.f, 0.f};
      LAS char* ap = sm + L_PK + (4 * g + q4) * 160 + h * 80 + 8 * p4;
      LAS char* bp = sm + L_V + (4 * g + q4) * 96 + h * 48 + 32 * nt + 8 * p4;
      u32x4 B[4], A[4][3];
#pragma unroll
      for (int k4 = 0; k4 < 4; k4++) {
        B[k4] = trd2(bp + k4 * 3072, 1536);
#pragma unroll
        for (int m = 0; m < 3; m++) A[k4][m] = trd2(ap + k4 * 5120 + 32 * m, 2560);
      }
      SB();
#pragma unroll
      for (int k4 = 0; k4 < 4; k4++)
#pragma unroll
        for (int m = 0; m < 3; m++) kk[m] = mfma(A[k4][m], B[k4], kk[m]);
      if (nt == 0 || c16 < 5) {
        const u32 a0 = WS_PART + (bj * 16 + bi) * PSTR + (h * 21 + 16 * nt + c16) * 80 + g * 8;
        __builtin_amdgcn_raw_buffer_store_b64((u32x2){san(pk2(kk[0][0], kk[0][1])), san(pk2(kk[0][2], kk[0][3]))}, wsr, a0, 0, 16);
        __builtin_amdgcn_raw_buffer_store_b64((u32x2){san(pk2(kk[1][0], kk[1][1])), san(pk2(kk[1][2], kk[1][3]))}, wsr, a0 + 32, 0, 16);
        if (g < 2) __builtin_amdgcn_raw_buffer_store_b64((u32x2){san(pk2(kk[2][0], kk[2][1])), san(pk2(kk[2][2], kk[2][3]))}, wsr, a0 + 64, 0, 16);
      }
    }
    STAMP(6);
    {
      const f32x2 sw2 = *(LAS f32x2*)(sm + L_SWR);
      const float swr = sw2[0] + sw2[1];
      const int t = g & 1;
      const float mm = t ? mean[1] : mean[0], rs = t ? rstd[1] : rstd[0], sxw = t ? acc[1][5][1] : acc[0][5][1];
      const float rr = rs * (sxw - mm * swr);
      const u32 rb = __float_as_uint(rr);
      const u32 key = san((rb & 0x80000000u) ? ~rb : (rb | 0x80000000u));
      const u32 mk = rowmax_u32(key);
      const u64 bal = __ballot(key == mk);
      const u32 rowbits = (u32)(bal >> (16 * g)) & 0xFFFFu;
      if (g < 2 && (int)__builtin_ctz(rowbits) == c16)
        __builtin_amdgcn_raw_buffer_store_b128((u32x4){key, (u32)(bi * 128 + (2 * v + t) * 16 + c16), san(__float_as_uint(mm)), san(__float_as_uint(rs))}, wsr, WS_CAND + bj * 2048 + (bi * 8 + 2 * v + t) * 16, 0, 16);
    }
    {
      const f32x4 cB = *(LAS f32x4*)(sm + L_CS + 5 * 64 + g * 16), cC = *(LAS f32x4*)(sm + L_CS + 6 * 64 + g * 16), cA = *(LAS f32x4*)(sm + L_CS + 7 * 64 + g * 16);
      u32x4 b0[2]; u32x2 qa[2];
#pragma unroll
      for (int t = 0; t < 2; t++) {
        const f32x4 vB = acc[t][6] - cB * mean[t], vC = acc[t][7] - cC * mean[t], vA = acc[t][8] - cA * mean[t];
        b0[t] = (u32x4){pk2(vB[0], vB[1]), pk2(vB[2], vB[3]), pk2(vC[0], vC[1]), pk2(vC[2], vC[3])};
        qa[t] = (u32x2){pk2(vA[0], vA[1]), pk2(vA[2], vA[3])};
      }
      *(LAS u32x4*)(sm + L_HO + v * 4096 + l * 16) = b0[1]; *(LAS u32x2*)(sm + L_HO + v * 4096 + 1024 + l * 8) = qa[1];
      cnt_signal(12 + v);
      phiq_tile(b0[0], qa[0], rstd[0]);
    }
    STAMP(7);
    if (v == 3) { check_flags(wsr, tag, l, tmo); cnt_signal(2); }
    {
      const int ch = v * 53 + l;
      const bool val = l < 53 && ch < 210;
      const u32 base = val ? WS_PART + (qj * 16) * PSTR + ch * 16 : OOB;
      cnt_wait(sm, 2, 1);
      STAMP(8);
      u32x4 pv[16]; u32 spins = 0;
#pragma unroll
      for (int p = 0; p < 16; p++) pv[p] = __builtin_amdgcn_raw_buffer_load_b128(wsr, base, p * PSTR, 16);
      float s[8];
#pragma unroll
      for (int e = 0; e < 8; e++) s[e] = 0.f;
#pragma unroll
      for (int p = 0; p < 16; p++) {
        while (!__all(ok4(pv[p]))) {
          asm volatile("" ::: "memory");
          if (++spins > SPIN_CAP) { tmo = true; break; }
#pragma unroll
          for (int p2 = p; p2 < 16; p2++) pv[p2] = __builtin_amdgcn_raw_buffer_load_b128(wsr, base, p2 * PSTR, 16);
        }
#pragma unroll
        for (int e = 0; e < 4; e++) { s[2 * e] += bflo(pv[p][e]); s[2 * e + 1] += bfhi(pv[p][e]); }
      }
      STAMP(9);
      if (val) {
        const int row = ch / 5, cq = ch % 5;
        const u32 a = L_KVR + row * 96 + cq * 16;
        *(LAS u32x2*)(sm + a + (cq > 2 ? 8 : 0)) = (u32x2){pk2(s[0], s[1]), pk2(s[2], s[3])};
        *(LAS u32x2*)(sm + a + 8 + (cq >= 2 ? 8 : 0)) = (u32x2){pk2(s[4], s[5]), pk2(s[6], s[7])};
      }
    }
    BARRIER_LDS();
    STAMP(10);
    O_STAGE();
    STAMP(11);
    {
      const int tp = v & 1, kh = v >> 1;
      const int ft = 2 * tp + kh;
      const int tok = bi * 128 + th * 64 + ft * 16 + c16;
      u32x2 xr[5];
      const u32x2* xp = (const u32x2*)(x + tok * 640 + co * 80 + 20 * g);
#pragma unroll
      for (int e = 0; e < 5; e++) xr[e] = xp[e];
      LAS char* wrd = kvrd + kh * 51200;
      u32x4 wf[2][5];
#pragma unroll
      for (int n = 0; n < 2; n++)
#pragma unroll
        for (int T = 0; T < 5; T++) wf[n][T] = trd2(wrd + n * 5120 + 8 * T, 2560);
      asm volatile("s_waitcnt vmcnt(0)" ::: "memory");
      const u32 ob = WS_O + ((bi * 8 + 4 * th + 2 * tp) * 80 + 40 * kh) * 256 + l * 16;
      u32x4 of[2][10]; u32 spins = 0;
#pragma unroll
      for (int n = 0; n < 10; n++) {
        of[0][n] = __builtin_amdgcn_raw_buffer_load_b128(wsr, ob, n * 1024, 16);
        of[1][n] = __builtin_amdgcn_raw_buffer_load_b128(wsr, ob + 20480, n * 1024, 16);
      }
      f32x4 oc[2][5];
#pragma unroll
      for (int T = 0; T < 5; T++) { oc[0][T] = (f32x4){0.f, 0.f, 0.f, 0.f}; oc[1][T] = (f32x4){0.f, 0.f, 0.f, 0.f}; }
#pragma unroll
      for (int n = 0; n < 10; n++) {
        while (!__all(ok4(of[0][n]) && ok4(of[1][n]))) {
          asm volatile("" ::: "memory");
          if (++spins > SPIN_CAP) { tmo = true; break; }
#pragma unroll
          for (int n2 = n; n2 < 10; n2++) {
            of[0][n2] = __builtin_amdgcn_raw_buffer_load_b128(wsr, ob, n2 * 1024, 16);
            of[1][n2] = __builtin_amdgcn_raw_buffer_load_b128(wsr, ob + 20480, n2 * 1024, 16);
          }
        }
        if (n == 0) STAMP(12);
        u32x4 cur[5];
#pragma unroll
        for (int T = 0; T < 5; T++) cur[T] = wf[n & 1][T];
        if (n + 2 < 10) {
#pragma unroll
          for (int T = 0; T < 5; T++) wf[n & 1][T] = trd2(wrd + (n + 2) * 5120 + 8 * T, 2560);
        }
#pragma unroll
        for (int T = 0; T < 5; T++) { oc[0][T] = mfma(cur[T], of[0][n], oc[0][T]); oc[1][T] = mfma(cur[T], of[1][n], oc[1][T]); }
#pragma unroll
        for (int i = 0; i < 10; i++) { SGB(0x8, 1); SGB(0x100, 1); }
        SB();
      }
#pragma unroll
      for (int T = 0; T < 5; T++) *(LAS f32x4*)(sm + L_RB + ((v * 5 + T) * 64 + l) * 16) = kh ? oc[0][T] : oc[1][T];
      cnt_signal(4 + v);
      STAMP(13);
      const f32x2 st = *(LAS f32x2*)(sm + L_STAT + (th * 64 + ft * 16 + c16) * 8);
      const float a1 = 1.f + st[1], a0 = -st[0] * st[1];
      cnt_wait(sm, 3, 1);
      cnt_wait(sm, 4 + (v ^ 2), 1);
      STAMP(14);
      const bool sel = *(LAS u32*)(sm + L_Y + 320) == (u32)tok;
      u32x2* op = (u32x2*)(out + tok * 640 + co * 80 + 20 * g);
#pragma unroll
      for (int T = 0; T < 5; T++) {
        f32x4 o2 = (kh ? oc[1][T] : oc[0][T]) + *(LAS f32x4*)(sm + L_RB + (((v ^ 2) * 5 + T) * 64 + l) * 16);
        if (sel) o2 += *(LAS f32x4*)(sm + L_Y + (20 * g + 4 * T) * 4);
        op[T] = (u32x2){pk2(bflo(xr[T].x) * a1 + a0 + o2[0], bfhi(xr[T].x) * a1 + a0 + o2[1]), pk2(bflo(xr[T].y) * a1 + a0 + o2[2], bfhi(xr[T].y) * a1 + a0 + o2[3])};
      }
    }
  }
  if (tmo && l == 0) *(u32*)(ws + WS_TMO) = 1u;
  STAMP(15);
}
}

extern "C" void kernel_launch(void* const* d_in, const int* in_sizes, int n_in, void* d_out, int out_size, void* d_ws, size_t ws_size, hipStream_t stream) {
  using namespace fk;
  (void)hipFuncSetAttribute((const void*)fused, hipFuncAttributeMaxDynamicSharedMemorySize, L_TOTAL);
  fused<<<256, 512, L_TOTAL, stream>>>((const u16*)d_in[0], (const u16*)d_in[1], (const u16*)d_in[2], (const u16*)d_in[3], (const u16*)d_in[5],
                                       (const u16*)d_in[6], (const u16*)d_in[7], (const u16*)d_in[8], (u16*)d_out, (char*)d_ws);
}
```

```cpp
#include <hip/hip_runtime.h>
typedef unsigned short u16;
typedef unsigned int u32;
typedef unsigned long long u64;
typedef short s16x4 __attribute__((ext_vector_type(4)));
typedef __bf16 bf16x2 __attribute__((ext_vector_type(2)));
typedef __bf16 bf16x8 __attribute__((ext_vector_type(8)));
typedef float f32x2 __attribute__((ext_vector_type(2)));
typedef float f32x4 __attribute__((ext_vector_type(4)));
typedef u32 u32x2 __attribute__((ext_vector_type(2)));
typedef u32 u32x4 __attribute__((ext_vector_type(4)));
#define LAS __attribute__((address_space(3)))
#define GAS __attribute__((address_space(1)))
#define DI __device__ __forceinline__

namespace fk {
constexpr u32 WS_EPOCH = 0;
constexpr u32 WS_FLAG = 4096;
constexpr u32 WS_TMO = 8192;
constexpr u32 WS_CAND = 16384;
constexpr u32 WS_FK = 65536;
constexpr u32 WS_FO = 98304;
constexpr u32 WS_GU = 131072;
constexpr u32 WS_YP = 393216;
constexpr u32 WS_PART = 524288;
constexpr u32 PSTR = 3584;
constexpr u32 WS_O = WS_PART + 1048576;
constexpr u32 WS_END = WS_O + 2621440;
constexpr u32 SLOT = 32768, QO = 10240, XO = 16384;
constexpr u32 L_EX = 131072, L_MISC = 136192, L_TOTAL = 163840;
constexpr u32 L_CS = L_MISC;
constexpr u32 L_ZERO = L_MISC + 1024;
constexpr u32 L_CNT = L_MISC + 1088;
constexpr u32 L_WT = L_MISC + 1152;
constexpr u32 L_STAT = L_MISC + 4992;
constexpr u32 L_Y = L_MISC + 6016;
constexpr u32 L_TAG = L_MISC + 6400;
constexpr u32 L_CNT2 = L_MISC + 6408;
constexpr u32 L_SQ = L_MISC + 6432;
constexpr u32 L_KVR = 143360;
constexpr u32 L_HO = 147456;
constexpr u32 L_PK = 102400, L_V = 122880;
constexpr u32 L_RB = 102400;
constexpr u32 SENT = 0xFFFFFFFFu;
constexpr u32 OOB = 0x7FFF0000u;
constexpr u32 SPIN_CAP = 1u << 17;
constexpr u32 ONES = 0x3F803F80u;

DI float bflo(u32 v) { return __uint_as_float(v << 16); }
DI float bfhi(u32 v) { return __uint_as_float(v & 0xffff0000u); }
DI u32 pk2(float a, float b) { f32x2 f = {a, b}; return __builtin_bit_cast(u32, __builtin_convertvector(f, bf16x2)); }
DI float dot2(u32 a, u32 b, float c) { return __builtin_amdgcn_fdot2_f32_bf16(__builtin_bit_cast(bf16x2, a), __builtin_bit_cast(bf16x2, b), c, false); }
DI f32x4 mfma(u32x4 a, u32x4 b, f32x4 c) { return __builtin_amdgcn_mfma_f32_16x16x32_bf16(__builtin_bit_cast(bf16x8, a), __builtin_bit_cast(bf16x8, b), c, 0, 0, 0); }
DI u32x2 trd(LAS char* p) { return __builtin_bit_cast(u32x2, __builtin_amdgcn_ds_read_tr16_b64_v4i16((LAS s16x4*)p)); }
DI u32x4 trd2(LAS char* p, int d) { u32x2 a = trd(p), b = trd(p + d); return (u32x4){a.x, a.y, b.x, b.y}; }
DI float xs16(float v) { auto r = __builtin_amdgcn_permlane16_swap(__float_as_uint(v), __float_as_uint(v), false, false); return __uint_as_float(r[0]) + __uint_as_float(r[1]); }
DI float xs32(float v) { auto r = __builtin_amdgcn_permlane32_swap(__float_as_uint(v), __float_as_uint(v), false, false); return __uint_as_float(r[0]) + __uint_as_float(r[1]); }
DI float xmax16(float v) { auto r = __builtin_amdgcn_permlane16_swap(__float_as_uint(v), __float_as_uint(v), false, false); return fmaxf(__uint_as_float(r[0]), __uint_as_float(r[1])); }
DI float xmin16(float v) { auto r = __builtin_amdgcn_permlane16_swap(__float_as_uint(v), __float_as_uint(v), false, false); return fminf(__uint_as_float(r[0]), __uint_as_float(r[1])); }
template <int CTRL> DI u32 dpp(u32 v) { return __builtin_amdgcn_update_dpp(v, v, CTRL, 0xF, 0xF, false); }
DI u32 umax(u32 a, u32 b) { return a > b ? a : b; }
DI u32 rowmax_u32(u32 k) { k = umax(k, dpp<0x128>(k)); k = umax(k, dpp<0x124>(k)); k = umax(k, dpp<0x122>(k)); k = umax(k, dpp<0x121>(k)); return k; }
DI float xs8(float v) { return v + __uint_as_float(dpp<0x128>(__float_as_uint(v))); }
DI u32 san(u32 v) { return v < 0xFFFFFFFEu ? v : 0xFFFFFFFEu; }
DI u32x4 san4(f32x4 v) { return (u32x4){san(__float_as_uint(v[0])), san(__float_as_uint(v[1])), san(__float_as_uint(v[2])), san(__float_as_uint(v[3]))}; }
DI u32 mx4(u32 m, u32x4 v) { return umax(umax(umax(m, v.x), v.y), umax(v.z, v.w)); }
DI bool anysent(u32 m) { return __builtin_amdgcn_ballot_w64(m == SENT) != 0; }
DI bool ok4(u32x4 v) { return v.x != SENT && v.y != SENT && v.z != SENT && v.w != SENT; }
DI void dma16(u32 voff, const void* sbase, u32 lds_dst) {
  asm volatile("s_mov_b32 m0, %2\n\ts_nop 0\n\tglobal_load_lds_dwordx4 %0, %1" :: "v"(voff), "s"(sbase), "s"(lds_dst) : "memory");
}
DI void dma16m(u32 voff, const void* sbase, u32 lds_dst, u64 mask) {
  asm volatile("s_mov_b32 m0, %2\n\ts_mov_b64 exec, %3\n\tglobal_load_lds_dwordx4 %0, %1\n\ts_mov_b64 exec, -1" :: "v"(voff), "s"(sbase), "s"(lds_dst), "s"(mask) : "memory");
}
template <int BIT> DI void cload(u32x4& dst, u32 todo, u32 voff, u32x4 rs, u32 soff) {
  asm volatile("s_bitcmp1_b32 %2, %5\n\ts_cbranch_scc0 1f\n\tbuffer_load_dwordx4 %0, %1, %3, %4 offen sc1\n1:"
               : "+v"(dst) : "v"(voff), "s"(todo), "s"(rs), "s"(soff), "i"(BIT) : "scc", "memory");
}
template <int N, int I = 0> struct CL { static DI void run(u32x4 (&d)[N], u32 todo, u32 voff, u32x4 rs, u32 stride) {
  if constexpr (I < N) { cload<I>(d[I], todo, voff, rs, I * stride); CL<N, I + 1>::run(d, todo, voff, rs, stride); } } };
DI void hload(u32& dst, u32 voff, u32x4 rs) { asm volatile("buffer_load_dword %0, %1, %2, 0 offen sc1" : "=v"(dst) : "v"(voff), "s"(rs) : "memory"); }
#define STAMP(n) do {} while (0)
#define SB() __builtin_amdgcn_sched_barrier(0)
#define SGB(m, n) __builtin_amdgcn_sched_group_barrier(m, n, 0)
#define PIN(x) asm volatile("" : "+v"(x))
#define BARRIER_LDS() asm volatile("s_waitcnt lgkmcnt(0)\n\ts_barrier" ::: "memory")
#define WAITB(N) asm volatile("s_waitcnt vmcnt(" #N ") lgkmcnt(0)\n\ts_barrier" ::: "memory")

DI void wait_until(u64 t) { while (__builtin_amdgcn_s_memrealtime() < t) __builtin_amdgcn_s_sleep(1); }
#define D_KV 80
#define D_LK 26
#define D_KVU 40
#define P_SLP 2
#define D_O 80
#define D_BK 30
DI void cnt_signal(int idx) {
  const u32 a = L_CNT + idx * 4, one = 1u;
  asm volatile("s_waitcnt lgkmcnt(0)\n\ts_mov_b64 exec, 1\n\tds_add_u32 %0, %1\n\ts_mov_b64 exec, -1" :: "v"(a), "v"(one) : "memory");
}
DI bool cnt_last(int idx, u32 n) {
  const u32 a = L_CNT2 + idx * 4, one = 1u; u32 r;
  asm volatile("s_mov_b64 exec, 1\n\tds_add_rtn_u32 %0, %1, %2\n\ts_waitcnt lgkmcnt(0)\n\ts_mov_b64 exec, -1" : "=&v"(r) : "v"(a), "v"(one) : "memory");
  return (u32)__builtin_amdgcn_readfirstlane(r) == n - 1;
}
DI void poll16(const __amdgpu_buffer_rsrc_t wsr, u32 base, u32 tag, int l, bool& tmo) {
  u32 spins = 0;
  for (;;) {
    asm volatile("" ::: "memory");
    const u32 f = __builtin_amdgcn_raw_buffer_load_b32(wsr, l < 16 ? base + l * 128 : OOB, 0, 16);
    if (((u32)__builtin_amdgcn_ballot_w64(f == tag) & 0xFFFFu) == 0xFFFFu) break;
    if (++spins > SPIN_CAP) { tmo = true; break; }
    __builtin_amdgcn_s_sleep(P_SLP);
  }
}
DI void cnt_wait(LAS char* sm, int idx, u32 target) {
  for (;;) {
    const u32 v = __builtin_amdgcn_readfirstlane(__hip_atomic_load((LAS u32*)(sm + L_CNT + idx * 4), __ATOMIC_RELAXED, __HIP_MEMORY_SCOPE_WORKGROUP));
    if (v >= target) break;
  }
  asm volatile("" ::: "memory");
}

DI void dual_softmax(const f32x4 (&p)[3], float rs, float os, u32 (&o)[10]) {
  const float sc = rs * 1.4426950408889634f;
  float mx = p[0][0], mn = p[0][0];
#pragma unroll
  for (int e = 1; e < 10; e++) { mx = fmaxf(mx, p[e >> 2][e & 3]); mn = fminf(mn, p[e >> 2][e & 3]); }
  mx = xmax16(mx) * sc; mn = xmin16(mn) * sc;
  float ep[10], en[10], sp = 0.f, sn = 0.f;
#pragma unroll
  for (int e = 0; e < 10; e++) {
    ep[e] = __builtin_amdgcn_exp2f(__builtin_fmaf(p[e >> 2][e & 3], sc, -mx)); en[e] = __builtin_amdgcn_exp2f(__builtin_fmaf(p[e >> 2][e & 3], -sc, mn));
    sp += ep[e]; sn += en[e];
  }
  sp = __builtin_amdgcn_rcpf(xs16(sp)) * os; sn = __builtin_amdgcn_rcpf(xs16(sn)) * os;
#pragma unroll
  for (int e = 0; e < 5; e++) { o[e] = pk2(ep[2 * e] * sp, ep[2 * e + 1] * sp); o[5 + e] = pk2(en[2 * e] * sn, en[2 * e + 1] * sn); }
}

struct Cand { u32 idx; float r, mean, rstd; };
DI Cand poll_cand(const __amdgpu_buffer_rsrc_t wsr, int rep, int l, bool& tmo) {
  u32x4 a, b; u32 spins = 0;
  for (;;) {
    asm volatile("" ::: "memory");
    a = __builtin_amdgcn_raw_buffer_load_b128(wsr, WS_CAND + rep * 2048 + l * 16, 0, 16);
    b = __builtin_amdgcn_raw_buffer_load_b128(wsr, WS_CAND + rep * 2048 + 1024 + l * 16, 0, 16);
    if (__all(ok4(a) && ok4(b))) break;
    if (++spins > SPIN_CAP) { tmo = true; break; }
    __builtin_amdgcn_s_sleep(2);
  }
  u32 mk = rowmax_u32(umax(a.x, b.x));
  { auto r = __builtin_amdgcn_permlane16_swap(mk, mk, false, false); mk = umax(r[0], r[1]); }
  { auto r = __builtin_amdgcn_permlane32_swap(mk, mk, false, false); mk = umax(r[0], r[1]); }
  const u64 ba = __ballot(a.x == mk), bbal = __ballot(b.x == mk);
  if (ba == 0) a = b;
  const int src = __builtin_ctzll((ba ? ba : bbal) | (1ull << 63));
  Cand c;
  c.idx = __builtin_amdgcn_readlane(a.y, src) & 2047u;
  c.r = __uint_as_float((mk & 0x80000000u) ? (mk & 0x7FFFFFFFu) : ~mk);
  c.mean = __uint_as_float(__builtin_amdgcn_readlane(a.z, src));
  c.rstd = __uint_as_float(__builtin_amdgcn_readlane(a.w, src));
  return c;
}
DI void check_flags(const __amdgpu_buffer_rsrc_t wsr, u32 tag, int l, bool& tmo) {
  u32 spins = 0;
  for (;;) {
    asm volatile("" ::: "memory");
    const u32x4 fl = __builtin_amdgcn_raw_buffer_load_b128(wsr, WS_FLAG + l * 16, 0, 16);
    if (__all(fl.x == tag && fl.y == tag && fl.z == tag && fl.w == tag)) break;
    if (++spins > SPIN_CAP) { tmo = true; break; }
    __builtin_amdgcn_s_sleep(1);
  }
}

__global__ __launch_bounds__(512, 2) void fused(const u16* __restrict__ x, const u16* __restrict__ wqkv, const u16* __restrict__ wphi,
                                                const u16* __restrict__ wo, const u16* __restrict__ wr, const u16* __restrict__ wg,
                                                const u16* __restrict__ wu, const u16* __restrict__ wd, u16* __restrict__ out, char* __restrict__ ws) {
  extern __shared__ __attribute__((aligned(16))) char smem_[];
  LAS char* sm = (LAS char*)smem_;
  STAMP(0);
  const int tid = threadIdx.x, l = tid & 63, c16 = l & 15, g = l >> 4;
  const int w = __builtin_amdgcn_readfirstlane(tid >> 6);
  const int v = w & 3;
  const int bid = blockIdx.x, xcd = bid & 7, slot = bid >> 3;
  const int bi = 4 * (xcd >> 1) + (slot & 3), bj = 8 * (xcd & 1) + (slot >> 2);
  const int r0 = (((slot >> 2) * 5 / 2 + (slot & 3) * 5) >> 1) % 10;
  const int qj = bj ^ 1, bq = bj >> 1;
  const int q4 = c16 >> 2, p4 = c16 & 3;
  const int th = bj & 1, co = bj >> 1;
  const int bb = bi * 16 + bj;
  const int fs = bb % 24, ks = bb / 24;
  const bool bon = bb < 240;
  const int tile = 2 * v + (w >> 2);
  const __amdgpu_buffer_rsrc_t wsr = __builtin_amdgcn_make_buffer_rsrc(ws, 0, WS_END, 0x00020000);
  bool tmo = false;
  const u32x4 wsq = {(u32)(u64)ws, (u32)((u64)ws >> 32), WS_END, 0x00020000u};
  LAS char* kvrd = sm + (4 * g + q4) * 160 + p4 * 40;
  LAS char* qrd = sm + QO + (4 * g + q4) * 96 + p4 * 8;
  LAS char* xrd0 = sm + XO + v * 4096 + (c16 >> 3) * 1024 + (c16 & 7) * 128 + ((g ^ (c16 >> 1)) * 16);
  LAS char* xrd1 = sm + XO + v * 4096 + (c16 >> 3) * 1024 + (c16 & 7) * 128 + (((4 + g) ^ (c16 >> 1)) * 16);
  u32 tag = 0;
  u32 phq[10];
  u32x4 a2k[3][3];
  u32 a2ad[3], oad[3];
  auto addr_pre = [&]() {
    const int hr = q4 >> 1;
#pragma unroll
    for (int t = 0; t < 3; t++) {
      const int fl_ = 4 * t + p4; const bool fv = fl_ < 10 && g == hr; const int f = 10 * (q4 & 1) + fl_;
      a2ad[t] = fv ? L_WT + ((2 * (bj & 1) + hr) * 20 + f) * 48 : L_ZERO;
      PIN(a2ad[t]);
      int row;
      if (t < 2) { const int c = 8 * q4 + 4 * t + p4; row = c + (c >= 20); }
      else row = p4 < 2 ? 33 + 2 * q4 + p4 : (p4 == 2 ? 20 : 41);
      oad[t] = (row >= 21) == (g >> 1) ? L_KVR + row * 96 + (g & 1) * 48 : L_ZERO;
      PIN(oad[t]);
    }
  };
  auto load_a2k = [&]() {
#pragma unroll
    for (int t = 0; t < 3; t++)
#pragma unroll
      for (int s2 = 0; s2 < 3; s2++) a2k[t][s2] = *(LAS u32x4*)(sm + a2ad[t] + 16 * s2);
  };
  auto phik_mma = [&](f32x4 (&pk)[3], u32x4 b0, u32x4 b1, u32x4 b2) {
#pragma unroll
    for (int t = 0; t < 3; t++) pk[t] = mfma(a2k[t][0], b0, (f32x4){0.f, 0.f, 0.f, 0.f});
#pragma unroll
    for (int t = 0; t < 3; t++) pk[t] = mfma(a2k[t][1], b1, pk[t]);
#pragma unroll
    for (int t = 0; t < 3; t++) pk[t] = mfma(a2k[t][2], b2, pk[t]);
  };
  auto phik_fin = [&](const f32x4 (&pk)[3], float rs) {
    u32 ph[10];
    dual_softmax(pk, rs, rs, ph);
    LAS u32x2* pp = (LAS u32x2*)(sm + L_PK + (tile * 16 + c16) * 160 + g * 40);
#pragma unroll
    for (int e = 0; e < 5; e++) pp[e] = (u32x2){ph[2 * e], ph[2 * e + 1]};
  };
  auto phiq_tile = [&](u32x4 b0, u32x2 qa, float rs) {
    u32x4 a0[3]; u32x2 a1[3];
    const int hr = q4 >> 1;
#pragma unroll
    for (int t = 0; t < 3; t++) {
      const int fl_ = 4 * t + p4; const bool fv = fl_ < 10; const int f = 10 * (q4 & 1) + fl_;
      const u32 rowb = L_WT + ((2 * (qj & 1) + hr) * 20 + f) * 48;
      const u32 alo = (fv && hr == 0) ? rowb + 8 * g : L_ZERO;
      const u32 ahi = (fv && ((g == 0) == (hr == 0))) ? rowb + (g == 0 ? 32 : 8 * (g - 1)) : L_ZERO;
      const u32 aa = (fv && hr == 1 && g < 2) ? rowb + 24 + 8 * g : L_ZERO;
      const u32x2 lo = *(LAS u32x2*)(sm + alo), hi = *(LAS u32x2*)(sm + ahi);
      a0[t] = (u32x4){lo.x, lo.y, hi.x, hi.y}; a1[t] = *(LAS u32x2*)(sm + aa);
    }
    SB();
    f32x4 pq[3];
#pragma unroll
    for (int t = 0; t < 3; t++) pq[t] = mfma(a0[t], b0, (f32x4){0.f, 0.f, 0.f, 0.f});
#pragma unroll
    for (int t = 0; t < 3; t++) pq[t] = mfma((u32x4){a1[t].x, a1[t].y, 0u, 0u}, (u32x4){qa.x, qa.y, 0u, 0u}, pq[t]);
    dual_softmax(pq, rs, 1.f, phq);
  };
  auto o_stage = [&]() {
    u32x4 A[3][3];
#pragma unroll
    for (int u = 0; u < 3; u++)
#pragma unroll
      for (int s3 = 0; s3 < 3; s3++) A[u][s3] = *(LAS u32x4*)(sm + oad[u] + 16 * s3);
    SB();
    f32x4 od[3];
#pragma unroll
    for (int u = 2; u >= 0; u--) od[u] = mfma(A[u][0], (u32x4){phq[0], phq[1], phq[2], phq[3]}, (f32x4){0.f, 0.f, 0.f, 0.f});
#pragma unroll
    for (int u = 2; u >= 0; u--) od[u] = mfma(A[u][1], (u32x4){phq[4], phq[5], phq[6], phq[7]}, od[u]);
#pragma unroll
    for (int u = 2; u >= 0; u--) od[u] = mfma(A[u][2], (u32x4){phq[8], phq[9], 0u, 0u}, od[u]);
    const float ia = __builtin_amdgcn_rcpf(od[2][2] + 1e-6f), ib = __builtin_amdgcn_rcpf(od[2][3] + 1e-6f);
    const float i0 = g <= 2 ? ia : ib, i1 = g <= 1 ? ia : ib;
    const u32 ob = WS_O + ((bi * 8 + tile) * 80 + 5 * qj) * 256;
    __builtin_amdgcn_raw_buffer_store_b128((u32x4){san(pk2(od[0][0] * i0, od[0][1] * i0)), san(pk2(od[0][2] * i0, od[0][3] * i0)), san(pk2(od[1][0] * i1, od[1][1] * i1)), san(pk2(od[1][2] * i1, od[1][3] * i1))}, wsr, ob + l * 16, 0, 16);
    __builtin_amdgcn_raw_buffer_store_b32(san(pk2(od[2][0] * ib, od[2][1] * ib)), wsr, ob + 1024 + c16 * 16 + 4 * g, 0, 16);
    if (cnt_last(1, 8) && l == 0) __builtin_amdgcn_raw_buffer_store_b32(tag, wsr, WS_FO + (bi * 16 + qj) * 128, 0, 16);
  };

  if (w >= 4) {
    __builtin_amdgcn_s_setprio(3);
    const int r8 = l >> 3;
    const u32 xoff = (bi * 128 + v * 32 + r8) * 1280 + (((l & 7) ^ (r8 >> 1)) * 16);
    auto rotd = [&](int c) { int d = c + r0; return d >= 10 ? d - 10 : d; };
    auto issue_x = [&](int c) {
      const char* b = (const char*)x + rotd(c) * 128;
      const u32 dst = (c & 3) * SLOT + XO + v * 4096;
      dma16(xoff, b, dst); dma16((xoff ^ 64u) + 8 * 1280, b, dst + 1024);
      dma16(xoff + 16 * 1280, b, dst + 2048); dma16((xoff ^ 64u) + 24 * 1280, b, dst + 3072);
    };
    issue_x(0);
    u32 wvoff[3], wqoff[2]; u64 qm[2];
    const int kv0 = (v >> 1) * 32 + (v & 1) * 4;
#pragma unroll
    for (int u = 0; u < 3; u++) {
      const int c = 64 * u + (u == 2 ? (l & 31) : l), rl = c / 10, cc = c % 10;
      wvoff[u] = (kv0 + 8 * (rl >> 2) + (rl & 3)) * 3840 + (cc < 5 ? 1280 + 16 * cc : 2560 + 16 * (cc - 5)) + 80 * bj;
    }
#pragma unroll
    for (int u = 0; u < 2; u++) {
      const int c = 64 * u + (u == 1 ? (l & 31) : l), rl = c / 6, cc = c % 6;
      wqoff[u] = (kv0 + 8 * (rl >> 2) + (rl & 3)) * 3840 + 80 * qj + 16 * cc;
      qm[u] = __ballot(cc < 5 && (u == 0 || l < 32));
    }
    auto issue_w = [&](int c) {
      const char* wb = (const char*)wqkv + rotd(c) * 245760;
      const u32 sb = (c & 3) * SLOT;
      dma16(wvoff[0], wb, sb + v * 2560); dma16(wvoff[1], wb, sb + v * 2560 + 1024); dma16m(wvoff[2], wb, sb + v * 2560 + 2048, 0xFFFFFFFFull);
      dma16m(wqoff[0], wb, sb + QO + v * 1536, qm[0]); dma16m(wqoff[1], wb, sb + QO + v * 1536 + 1024, qm[1]);
    };
    issue_w(0); issue_x(1); issue_w(1);
    STAMP(1);
    u32 ca[5], exv[6], pfw; u32x4 bw[8];
    f32x4 csa = {0.f, 0.f, 0.f, 0.f}, csb = {0.f, 0.f, 0.f, 0.f};
    float sq[2] = {0.f, 0.f};
    const u32x4 ones4 = {ONES, ONES, ONES, ONES};
    LAS char* csrd = kvrd + 8 * v;
    LAS char* cs2a = v == 0 ? kvrd + 32 : qrd + 32 * (v - 1);
    LAS char* cs2b = cs2a + (v == 0 ? 2560 : 1536);
    LAS char* cs2c = cs2a + (v == 0 ? 5120 : 3072);
    LAS char* cs2d = cs2b + (v == 0 ? 5120 : 3072);
    u32x4 xv[4];
#pragma unroll
    for (int c = 0; c < 10; c++) {
      if (c == 0) WAITB(9); else if (c <= 6) WAITB(18); else if (c == 7) WAITB(23); else if (c == 8) WAITB(23); else WAITB(9);
      if (c == 0) STAMP(2);
      const u32 sb = (c & 3) * SLOT;
      if (c == 0) { issue_x(2); issue_w(2); }
      if (c <= 6) { issue_x(c + 3); issue_w(c + 3); }
      if (c >= 1 && c <= 6) { if (l < 16) *(LAS u32*)(sm + ((c + 3) & 3) * SLOT + QO + (16 * v + l) * 96 + 80) = exv[c - 1]; }
      if (c == 6) {
        const u32x4 sv = {SENT, SENT, SENT, SENT};
#pragma unroll
        for (int n = 0; n < 5; n++) __builtin_amdgcn_raw_buffer_store_b128(sv, wsr, ca[n], 0, 16);
      }
      if (c == 7) {
        const char* b0 = (const char*)x; const char* b1 = (const char*)x; u32 o = l * 16, st = 1024;
        if (bon && (v == 1 || v == 2)) { b0 = (const char*)wg; b1 = (const char*)wu; o = ((ks * 64 + (v - 1) * 32 + (l >> 3)) * 1536 + fs * 64 + (l & 7) * 8) * 2; st = 8 * 1536 * 2; }
        if (bon && v == 0) { b0 = (const char*)wd; b1 = b0 + 32 * 640 * 2; o = ((fs * 64 + (l >> 3)) * 640 + ks * 64 + (l & 7) * 8) * 2; st = 8 * 640 * 2; }
#pragma unroll
        for (int n = 0; n < 4; n++) {
          asm volatile("global_load_dwordx4 %0, %1, %2" : "=v"(bw[n]) : "v"(o + n * st), "s"(b0) : "memory");
          asm volatile("global_load_dwordx4 %0, %1, %2" : "=v"(bw[4 + n]) : "v"(o + n * st), "s"(b1) : "memory");
        }
        const int e = l < 25 ? l : 0;
        const u32 po = (slot * 20 + v * 5 + e / 5) * 1280 + (xcd & 1) * 640 + (e % 5) * 128;
        asm volatile("global_load_dword %0, %1, %2" : "=v"(pfw) : "v"(po), "s"(wo) : "memory");
      }
      if (c == 9 && tid == 256) __hip_atomic_store((GAS u32*)(ws + WS_FLAG) + bid, tag, __ATOMIC_RELAXED, __HIP_MEMORY_SCOPE_AGENT);
      SB();
      const u32x4 c0 = trd2(csrd + sb, 2560), c1 = trd2(csrd + sb + 5120, 2560);
      const u32x2 d0 = trd(cs2a + sb), d1 = trd(cs2b + sb), d2 = trd(cs2c + sb), d3 = trd(cs2d + sb);
#pragma unroll
      for (int t = 0; t < 2; t++) { xv[2 * t] = *(LAS u32x4*)(xrd0 + sb + t * 2048); xv[2 * t + 1] = *(LAS u32x4*)(xrd1 + sb + t * 2048); }
      if (c == 0) {
#pragma unroll
        for (int n = 0; n < 6; n++) exv[n] = *(LAS u32*)(sm + L_EX + (n * 64 + 16 * v + c16) * 4);
      }
      if (c == 7) { load_a2k(); tag = __builtin_amdgcn_readfirstlane(*(LAS u32*)(sm + L_TAG)); }
      SB();
      csa = mfma(c0, ones4, csa);
      csb = mfma((u32x4){d0.x, d0.y, d1.x, d1.y}, ones4, csb);
      csa = mfma(c1, ones4, csa);
      csb = mfma((u32x4){d2.x, d2.y, d3.x, d3.y}, ones4, csb);
      if (c < 9) {
#pragma unroll
        for (int t = 0; t < 2; t++)
#pragma unroll
          for (int m = 0; m < 4; m++) { sq[t] = dot2(xv[2 * t][m], xv[2 * t][m], sq[t]); sq[t] = dot2(xv[2 * t + 1][m], xv[2 * t + 1][m], sq[t]); }
        PIN(sq[0]); PIN(sq[1]);
      }
      PIN(csa); PIN(csb);
      if (c == 2) addr_pre();
      if (c == 3) {
        const int ht = tid - 256;
        ca[0] = ht < 224 ? WS_PART + (bj * 16 + bi) * PSTR + ht * 16 : OOB;
        const u32 ob = WS_O + ((bi * 8 + 2 * v) * 80 + 5 * qj) * 256;
        ca[1] = ob + l * 16; ca[2] = ob + 20480 + l * 16;
        ca[3] = l < 32 ? ob + (l >> 4) * 20480 + 1024 + c16 * 16 : OOB;
        u32 a = OOB;
        if (ht < 8) a = WS_CAND + bj * 2048 + (bi * 8 + ht) * 16;
        else if (ht >= 64 && ht < 128 && bon) a = WS_GU + (fs * 20 + ks * 2) * 512 + (ht - 64) * 16;
        else if (ht >= 128 && ht < 144 && bon) a = WS_YP + fs * 2560 + ks * 256 + (ht - 128) * 16;
        ca[4] = a;
#pragma unroll
        for (int n = 0; n < 5; n++) PIN(ca[n]);
      }
      SB();
    }
    if (c16 == 0) { *(LAS f32x4*)(sm + L_CS + v * 64 + g * 16) = csa; *(LAS f32x4*)(sm + L_CS + (4 + v) * 64 + g * 16) = csb; }
    BARRIER_LDS();
    STAMP(3);
    __builtin_amdgcn_s_setprio(0);
#pragma unroll
    for (int t = 0; t < 2; t++)
#pragma unroll
      for (int m = 0; m < 4; m++) { sq[t] = dot2(xv[2 * t][m], xv[2 * t][m], sq[t]); sq[t] = dot2(xv[2 * t + 1][m], xv[2 * t + 1][m], sq[t]); }
    sq[0] = xs32(xs16(sq[0])); sq[1] = xs32(xs16(sq[1]));
    if (g < 2) *(LAS float*)(sm + L_SQ + ((2 * v + g) * 16 + c16) * 4) = g == 0 ? sq[0] : sq[1];
    cnt_signal(4 + v);
    cnt_wait(sm, 8 + v, 1);
    float rs1;
    {
      const u32x4 b0 = *(LAS u32x4*)(sm + L_HO + v * 4096 + l * 16), b1 = *(LAS u32x4*)(sm + L_HO + v * 4096 + 1024 + l * 16);
      const u32x4 b2 = *(LAS u32x4*)(sm + L_HO + v * 4096 + 2048 + l * 16);
      const f32x4 ex = *(LAS f32x4*)(sm + L_HO + v * 4096 + 3072 + c16 * 16);
      const float swr = *(LAS float*)(sm + L_CS + 7 * 64 + 9 * 4);
      f32x4 pk[3];
      phik_mma(pk, b0, b1, (u32x4){b2.x, b2.y, 0u, 0u});
      const float m1 = __uint_as_float(b2.z);
      rs1 = __builtin_amdgcn_rsqf(fmaxf(sq[1] * (1.f / 640.f) - m1 * m1, 0.f) + 1e-5f);
      phik_fin(pk, rs1);
      cnt_signal(0);
      STAMP(4);
      const int t = g & 1;
      const float mm = (t ? ex[2] : ex[0]) * (1.f / 640.f), sxw = t ? ex[3] : ex[1];
      const float rs = __builtin_amdgcn_rsqf(fmaxf((t ? sq[1] : sq[0]) * (1.f / 640.f) - mm * mm, 0.f) + 1e-5f);
      if (g < 2) *(LAS f32x2*)(sm + L_STAT + ((2 * v + g) * 16 + c16) * 8) = (f32x2){mm, rs};
      const float rr = rs * (sxw - mm * swr);
      const u32 rb = __float_as_uint(rr);
      const u32 key = san((rb & 0x80000000u) ? ~rb : (rb | 0x80000000u));
      const u32 mk = rowmax_u32(key);
      const u64 bal = __ballot(key == mk);
      const u32 rowbits = (u32)(bal >> (16 * g)) & 0xFFFFu;
      if (g < 2 && (int)__builtin_ctz(rowbits) == c16)
        __builtin_amdgcn_raw_buffer_store_b128((u32x4){key, (u32)(bi * 128 + (2 * v + t) * 16 + c16), san(__float_as_uint(mm)), san(__float_as_uint(rs))}, wsr, WS_CAND + bj * 2048 + (bi * 8 + 2 * v + t) * 16, 0, 16);
    }
    asm volatile("s_waitcnt vmcnt(0)" : "+v"(bw[0]), "+v"(bw[1]), "+v"(bw[2]), "+v"(bw[3]), "+v"(bw[4]), "+v"(bw[5]), "+v"(bw[6]), "+v"(bw[7]), "+v"(pfw) :: "memory");
    if (v == 1) { check_flags(wsr, tag, l, tmo); cnt_signal(2); }
    if (v == 0 || v == 3) {
      u32 wooff[3];
#pragma unroll
      for (int u = 0; u < 3; u++) {
        const int c = 64 * u + (u == 2 ? (l & 31) : l), rl = c / 10, cc = c % 10;
        wooff[u] = ((v >> 1) * 32 + 8 * (rl >> 2) + (rl & 3)) * 1280 + co * 160 + 16 * cc;
      }
#pragma unroll
      for (int s = 0; s < 10; s++)
#pragma unroll
        for (int hq = 0; hq < 2; hq++) {
          const char* wb = (const char*)wo + s * 81920 + hq * 5120;
          const u32 dst = s * 10240 + ((v >> 1) * 2 + hq) * 2560;
          dma16(wooff[0], wb, dst); dma16(wooff[1], wb, dst + 1024); dma16m(wooff[2], wb, dst + 2048, 0xFFFFFFFFull);
        }
    }
    STAMP(5);
    cnt_wait(sm, 12 + v, 1);
    phiq_tile(*(LAS u32x4*)(sm + L_HO + v * 4096 + l * 16), *(LAS u32x2*)(sm + L_HO + v * 4096 + 1024 + l * 8), rs1);
    STAMP(6);
    cnt_wait(sm, 2, 1);
    STAMP(7);
    if ((v == 1 || v == 2) && bon) {
      const Cand cd = poll_cand(wsr, bj, l, tmo);
      float ag[8], au[8];
#pragma unroll
      for (int e = 0; e < 8; e++) { ag[e] = 0.f; au[e] = 0.f; }
      const u16* xr = x + cd.idx * 640 + ks * 64 + (v - 1) * 32 + (l >> 3);
#pragma unroll
      for (int n = 0; n < 4; n++) {
        const float xs = (bflo(xr[n * 8]) - cd.mean) * cd.rstd;
#pragma unroll
        for (int e = 0; e < 4; e++) {
          ag[2 * e] += xs * bflo(bw[n][e]); ag[2 * e + 1] += xs * bfhi(bw[n][e]);
          au[2 * e] += xs * bflo(bw[4 + n][e]); au[2 * e + 1] += xs * bfhi(bw[4 + n][e]);
        }
      }
#pragma unroll
      for (int e = 0; e < 8; e++) { ag[e] = xs32(xs16(xs8(ag[e]))); au[e] = xs32(xs16(xs8(au[e]))); }
      const int sel = l >> 3;
      if (sel < 4) {
        f32x4 vv;
        if (sel == 0) vv = (f32x4){ag[0], ag[1], ag[2], ag[3]}; else if (sel == 1) vv = (f32x4){ag[4], ag[5], ag[6], ag[7]};
        else if (sel == 2) vv = (f32x4){au[0], au[1], au[2], au[3]}; else vv = (f32x4){au[4], au[5], au[6], au[7]};
        __builtin_amdgcn_raw_buffer_store_b128(san4(vv), wsr, WS_GU + (fs * 20 + ks * 2 + v - 1) * 512 + (sel >> 1) * 256 + (l & 7) * 32 + (sel & 1) * 16, 0, 16);
      }
    } else {
      asm volatile("s_waitcnt vmcnt(0)" ::: "memory");
    }
    STAMP(8);
    BARRIER_LDS();
    STAMP(10);
    o_stage();
    STAMP(11);
    if (v == 0 && bon) {
      const u32 base = WS_GU + fs * 20 * 512 + l * 4;
      u32 vg[20], vu[20]; u32 spins = 0;
      for (;;) {
        asm volatile("" ::: "memory");
        u32 m = 0;
#pragma unroll
        for (int p = 0; p < 20; p++) { vg[p] = __builtin_amdgcn_raw_buffer_load_b32(wsr, base, p * 512, 16); vu[p] = __builtin_amdgcn_raw_buffer_load_b32(wsr, base + 256, p * 512, 16); m = umax(umax(m, vg[p]), vu[p]); }
        if (!anysent(m)) break;
        if (++spins > SPIN_CAP) { tmo = true; break; }
        __builtin_amdgcn_s_sleep(8);
      }
      float gg = 0.f, uu = 0.f;
#pragma unroll
      for (int p = 0; p < 20; p++) { gg += __uint_as_float(vg[p]); uu += __uint_as_float(vu[p]); }
      const float hv = gg * __builtin_amdgcn_rcpf(1.f + __expf(-gg)) * uu;
      float y[8];
#pragma unroll
      for (int e = 0; e < 8; e++) y[e] = 0.f;
#pragma unroll
      for (int n = 0; n < 8; n++) {
        const float hh = __shfl(hv, (l >> 3) + 8 * n);
#pragma unroll
        for (int e = 0; e < 4; e++) { y[2 * e] += hh * bflo(bw[n][e]); y[2 * e + 1] += hh * bfhi(bw[n][e]); }
      }
#pragma unroll
      for (int e = 0; e < 8; e++) y[e] = xs32(xs16(xs8(y[e])));
      if (l < 16) {
        const f32x4 v4 = l < 8 ? (f32x4){y[0], y[1], y[2], y[3]} : (f32x4){y[4], y[5], y[6], y[7]};
        __builtin_amdgcn_raw_buffer_store_b128(san4(v4), wsr, WS_YP + fs * 2560 + ks * 256 + (l & 7) * 32 + (l >> 3) * 16, 0, 16);
      }
    }
    if (v == 3) {
      const Cand cd = poll_cand(wsr, bj, l, tmo);
      const bool mine = (int)(cd.idx >> 6) == bi * 2 + th;
      if (mine) {
        const int grp = l / 20, c4 = l % 20;
        const u32 base = grp < 3 ? WS_YP + (grp * 8) * 2560 + (co * 80 + c4 * 4) * 4 : OOB;
        u32x4 vv[8]; u32 sp2 = 0;
        for (;;) {
          asm volatile("" ::: "memory");
          u32 m = 0;
#pragma unroll
          for (int p = 0; p < 8; p++) { vv[p] = __builtin_amdgcn_raw_buffer_load_b128(wsr, base, p * 2560, 16); m = mx4(m, vv[p]); }
          if (!anysent(m)) break;
          if (++sp2 > SPIN_CAP) { tmo = true; break; }
          __builtin_amdgcn_s_sleep(4);
        }
        f32x4 a = __builtin_bit_cast(f32x4, vv[0]);
#pragma unroll
        for (int p = 1; p < 8; p++) a += __builtin_bit_cast(f32x4, vv[p]);
        f32x4 t;
#pragma unroll
        for (int e = 0; e < 4; e++) t[e] = a[e] + __shfl(a[e], c4 + 20) + __shfl(a[e], c4 + 40);
        const float sg = __builtin_amdgcn_rcpf(1.f + __expf(-cd.r));
        if (l < 20) *(LAS f32x4*)(sm + L_Y + l * 16) = t * sg;
      }
      if (l == 0) *(LAS u32*)(sm + L_Y + 320) = mine ? cd.idx : 0xFFFFu;
      cnt_signal(3);
      if (bid == 0 && l == 0) __hip_atomic_store((GAS u32*)(ws + WS_EPOCH), tag, __ATOMIC_RELAXED, __HIP_MEMORY_SCOPE_AGENT);
    }
    STAMP(12);
  } else {
    {
      const u32x4 wrc = *(const u32x4*)(wr + (tid < 80 ? tid : 0) * 8);
      const u32x4 wpc = *(const u32x4*)(wphi + 1600 * bq + (tid < 200 ? tid : 0) * 8);
      const u32 ep_raw = __hip_atomic_load((GAS u32*)(ws + WS_EPOCH), __ATOMIC_RELAXED, __HIP_MEMORY_SCOPE_AGENT);
      if (tid < 32) *(LAS u32*)(sm + L_ZERO + tid * 4) = 0u;
      { LAS u32* pz = (LAS u32*)(sm + (tid >> 6) * SLOT + QO + (tid & 63) * 96 + 84); pz[0] = 0u; pz[1] = 0u; pz[2] = 0u; }
      if (tid < 80) *(LAS u32x2*)(sm + L_WT + tid * 48 + 40) = (u32x2){0u, 0u};
      if (tid < 84) *(LAS u32x2*)(sm + L_KVR + tid * 48 + 40) = (u32x2){0u, 0u};
      addr_pre();
      if (tid < 80) {
        const int k0 = tid * 8; int st = (k0 >> 6) - r0; st = st < 0 ? st + 10 : st;
        const int rb = (k0 & 32) + 4 * ((k0 >> 3) & 3);
        const u32 ab = st < 4 ? st * SLOT + QO + rb * 96 + 80 : L_EX + ((st - 4) * 64 + rb) * 4;
        const u32 rs_ = st < 4 ? 96u : 4u;
#pragma unroll
        for (int j = 0; j < 8; j++) {
          const u32 hv = (j & 1) ? (wrc[j >> 1] & 0xFFFF0000u) : (wrc[j >> 1] << 16);
          *(LAS u32*)(sm + ab + (16 * (j >> 2) + (j & 3)) * rs_) = 0x3F80u | hv;
        }
      }
      if (tid < 200) {
#pragma unroll
        for (int j = 0; j < 8; j++) {
          const int e = tid * 8 + j, h = e / 400, r = e % 400, d = r / 20, f = r % 20;
          *(LAS u16*)(sm + L_WT + (h * 20 + f) * 48 + d * 2) = (u16)((j & 1) ? (wpc[j >> 1] >> 16) : (wpc[j >> 1] & 0xFFFFu));
        }
      }
      tag = __builtin_amdgcn_readfirstlane(ep_raw) + 1u;
      if (tid == 0) { *(LAS u32*)(sm + L_TAG) = tag; *(LAS u32x2*)(sm + L_CNT2) = (u32x2){0u, 0u}; *(LAS u32x2*)(sm + L_CNT2 + 8) = (u32x2){0u, 0u}; }
    }
    STAMP(1);
    f32x4 acc[2][8];
#pragma unroll
    for (int T = 0; T < 8; T++) { acc[0][T] = (f32x4){0.f, 0.f, 0.f, 0.f}; acc[1][T] = (f32x4){0.f, 0.f, 0.f, 0.f}; }
    u32x4 fb[8], xb[2];
#pragma unroll
    for (int c = 0; c < 10; c++) {
      BARRIER_LDS();
      if (c == 0) STAMP(2);
      const u32 sb = (c & 3) * SLOT;
      u32x4 fa[8], xa[2];
#pragma unroll
      for (int t = 0; t < 2; t++) xa[t] = *(LAS u32x4*)(xrd0 + sb + t * 2048);
#pragma unroll
      for (int T = 0; T < 5; T++) fa[T] = trd2(kvrd + sb + 8 * T, 2560);
#pragma unroll
      for (int T = 0; T < 3; T++) fa[5 + T] = trd2(qrd + sb + 32 * T, 1536);
      if (c > 0) {
#pragma unroll
        for (int T = 0; T < 8; T++) { acc[0][T] = mfma(fb[T], xb[0], acc[0][T]); acc[1][T] = mfma(fb[T], xb[1], acc[1][T]); }
#pragma unroll
        for (int i = 0; i < 2; i++) { SGB(0x8, 1); SGB(0x100, 2); }
#pragma unroll
        for (int i = 0; i < 14; i++) { SGB(0x8, 1); SGB(0x100, 1); }
      }
      SB();
#pragma unroll
      for (int t = 0; t < 2; t++) xb[t] = *(LAS u32x4*)(xrd1 + sb + t * 2048);
#pragma unroll
      for (int T = 0; T < 5; T++) fb[T] = trd2(kvrd + sb + 5120 + 8 * T, 2560);
#pragma unroll
      for (int T = 0; T < 3; T++) fb[5 + T] = trd2(qrd + sb + 3072 + 32 * T, 1536);
#pragma unroll
      for (int T = 0; T < 8; T++) { acc[0][T] = mfma(fa[T], xa[0], acc[0][T]); acc[1][T] = mfma(fa[T], xa[1], acc[1][T]); }
#pragma unroll
      for (int i = 0; i < 7; i++) { SGB(0x8, 1); SGB(0x100, 2); }
#pragma unroll
      for (int i = 0; i < 4; i++) { SGB(0x8, 1); SGB(0x100, 1); }
      SGB(0x8, 5);
      SB();
      if (c == 9) {
        load_a2k();
        acc[1][7] = mfma(fb[7], xb[1], acc[1][7]); acc[0][7] = mfma(fb[7], xb[0], acc[0][7]);
#pragma unroll
        for (int T = 0; T < 5; T++) acc[1][T] = mfma(fb[T], xb[1], acc[1][T]);
#pragma unroll
        for (int T = 0; T < 5; T++) acc[0][T] = mfma(fb[T], xb[0], acc[0][T]);
        SB();
      }
    }
    BARRIER_LDS();
    STAMP(3);
    float mean[2], rs0;
    {
      f32x4 cs[5];
#pragma unroll
      for (int T = 0; T < 5; T++) cs[T] = *(LAS f32x4*)(sm + L_CS + T * 64 + g * 16);
      if (g == 2) *(LAS f32x4*)(sm + L_HO + v * 4096 + 3072 + c16 * 16) = (f32x4){acc[0][7][0], acc[0][7][1], acc[1][7][0], acc[1][7][1]};
#pragma unroll
      for (int t = 0; t < 2; t++) mean[t] = xs32(xs16(g == 2 ? acc[t][7][0] : 0.f)) * (1.f / 640.f);
      SB();
      u32 P[2][10];
#pragma unroll
      for (int t = 1; t >= 0; t--) {
#pragma unroll
        for (int T = 0; T < 5; T++) {
          const f32x4 vv = acc[t][T] - cs[T] * mean[t];
          P[t][2 * T] = pk2(vv[0], vv[1]); P[t][2 * T + 1] = pk2(vv[2], vv[3]);
        }
        if (t == 1) {
          *(LAS u32x4*)(sm + L_HO + v * 4096 + l * 16) = (u32x4){P[1][0], P[1][1], P[1][2], P[1][3]};
          *(LAS u32x4*)(sm + L_HO + v * 4096 + 1024 + l * 16) = (u32x4){P[1][4], P[1][5], P[1][6], P[1][7]};
          *(LAS u32x4*)(sm + L_HO + v * 4096 + 2048 + l * 16) = (u32x4){P[1][8], P[1][9], __float_as_uint(mean[1]), 0u};
          cnt_signal(8 + v);
        }
      }
      f32x4 pk[3];
      phik_mma(pk, (u32x4){P[0][0], P[0][1], P[0][2], P[0][3]}, (u32x4){P[0][4], P[0][5], P[0][6], P[0][7]}, (u32x4){P[0][8], P[0][9], 0u, 0u});
#pragma unroll
      for (int T = 5; T < 7; T++) { acc[1][T] = mfma(fb[T], xb[1], acc[1][T]); acc[0][T] = mfma(fb[T], xb[0], acc[0][T]); }
      cnt_wait(sm, 4 + v, 1);
      const float s2a = *(LAS float*)(sm + L_SQ + ((2 * v) * 16 + c16) * 4), s2b = *(LAS float*)(sm + L_SQ + ((2 * v + 1) * 16 + c16) * 4);
      const float va = fmaxf(s2a * (1.f / 640.f) - mean[0] * mean[0], 0.f) + 1e-5f, vb = fmaxf(s2b * (1.f / 640.f) - mean[1] * mean[1], 0.f) + 1e-5f;
      rs0 = __builtin_amdgcn_rsqf(va);
      const float sd[2] = {va * rs0, vb * __builtin_amdgcn_rsqf(vb)};
      if (g >= 2) {
#pragma unroll
        for (int t = 0; t < 2; t++) {
          LAS u32x4* vp = (LAS u32x4*)(sm + L_V + ((2 * v + t) * 16 + c16) * 96 + (g - 2) * 48);
          vp[0] = (u32x4){P[t][0], P[t][1], P[t][2], P[t][3]}; vp[1] = (u32x4){P[t][4], P[t][5], P[t][6], P[t][7]}; vp[2] = (u32x4){P[t][8], P[t][9], pk2(sd[t], 0.f), 0u};
        }
      }
      phik_fin(pk, rs0);
    }
    cnt_signal(0);
    STAMP(4);
    cnt_wait(sm, 0, 8);
    STAMP(5);
    {
      const int h = v >> 1, nt = v & 1;
      f32x4 kk[3];
#pragma unroll
      for (int m = 0; m < 3; m++) kk[m] = (f32x4){0.f, 0.f, 0.f, 0.f};
      LAS char* ap = sm + L_PK + (4 * g + q4) * 160 + h * 80 + 8 * p4;
      LAS char* bp = sm + L_V + (4 * g + q4) * 96 + h * 48 + 32 * nt + 8 * p4;
      u32x4 B[4], A[4][3];
#pragma unroll
      for (int k4 = 0; k4 < 4; k4++) {
        B[k4] = trd2(bp + k4 * 3072, 1536);
#pragma unroll
        for (int m = 0; m < 3; m++) A[k4][m] = trd2(ap + k4 * 5120 + 32 * m, 2560);
      }
      SB();
#pragma unroll
      for (int k4 = 0; k4 < 4; k4++)
#pragma unroll
        for (int m = 0; m < 3; m++) kk[m] = mfma(A[k4][m], B[k4], kk[m]);
      if (nt == 0 || c16 < 5) {
        const u32 a0 = WS_PART + (bj * 16 + bi) * PSTR + (h * 21 + 16 * nt + c16) * 80 + g * 8;
        __builtin_amdgcn_raw_buffer_store_b64((u32x2){san(pk2(kk[0][0], kk[0][1])), san(pk2(kk[0][2], kk[0][3]))}, wsr, a0, 0, 16);
        __builtin_amdgcn_raw_buffer_store_b64((u32x2){san(pk2(kk[1][0], kk[1][1])), san(pk2(kk[1][2], kk[1][3]))}, wsr, a0 + 32, 0, 16);
        if (g < 2) __builtin_amdgcn_raw_buffer_store_b64((u32x2){san(pk2(kk[2][0], kk[2][1])), san(pk2(kk[2][2], kk[2][3]))}, wsr, a0 + 64, 0, 16);
      }
    }
    if (cnt_last(0, 4) && l == 0) __builtin_amdgcn_raw_buffer_store_b32(tag, wsr, WS_FK + (bj * 16 + bi) * 128, 0, 16);
    STAMP(6);
    {
      const f32x4 cB = *(LAS f32x4*)(sm + L_CS + 5 * 64 + g * 16), cC = *(LAS f32x4*)(sm + L_CS + 6 * 64 + g * 16), cA = *(LAS f32x4*)(sm + L_CS + 7 * 64 + g * 16);
      u32x4 b0[2]; u32x2 qa[2];
#pragma unroll
      for (int t = 0; t < 2; t++) {
        const f32x4 vB = acc[t][5] - cB * mean[t], vC = acc[t][6] - cC * mean[t], vA = acc[t][7] - cA * mean[t];
        b0[t] = (u32x4){pk2(vB[0], vB[1]), pk2(vB[2], vB[3]), pk2(vC[0], vC[1]), pk2(vC[2], vC[3])};
        qa[t] = (u32x2){pk2(vA[0], vA[1]), pk2(vA[2], vA[3])};
      }
      *(LAS u32x4*)(sm + L_HO + v * 4096 + l * 16) = b0[1]; *(LAS u32x2*)(sm + L_HO + v * 4096 + 1024 + l * 8) = qa[1];
      cnt_signal(12 + v);
      phiq_tile(b0[0], qa[0], rs0);
    }
    STAMP(7);
    {
      const int ch = v * 53 + l;
      const bool val = l < 53 && ch < 210;
      const u32 base = val ? WS_PART + (qj * 16) * PSTR + ch * 16 : OOB;
      const int row = ch / 5, cq = ch % 5;
      u32 ka = L_KVR + row * 96 + cq * 16 + (cq > 2 ? 8 : 0), kb = L_KVR + row * 96 + cq * 16 + 8 + (cq >= 2 ? 8 : 0);
      PIN(ka); PIN(kb);
      if (v == 0) { poll16(wsr, WS_FK + qj * 2048, tag, l, tmo); cnt_signal(1); } else cnt_wait(sm, 1, 1);
      STAMP(8);
      u32x4 pv[16]; u32 spins = 0;
      f32x2 s[4];
#pragma unroll
      for (int e = 0; e < 4; e++) s[e] = (f32x2){0.f, 0.f};
#pragma unroll
      for (int p = 0; p < 16; p++) pv[p] = __builtin_amdgcn_raw_buffer_load_b128(wsr, base, p * PSTR, 16);
      u32 todo = 0;
#pragma unroll
      for (int p = 0; p < 16; p++) {
        const bool bad = anysent(mx4(0u, pv[p]));
        todo |= bad ? (1u << p) : 0u;
        const float k = bad ? 0.f : 1.f;
        const u32 km = bad ? 0u : 0xFFFF0000u; const u32 sh = bad ? 0u : 0xFFFFFFFFu;
        (void)k;
#pragma unroll
        for (int e = 0; e < 4; e++) s[e] += (f32x2){__uint_as_float((pv[p][e] & sh) << 16), __uint_as_float(pv[p][e] & km)};
      }
      while (todo) {
        if (++spins > SPIN_CAP) { tmo = true; break; }
        wait_until(__builtin_amdgcn_s_memrealtime() + D_BK);
        CL<16>::run(pv, todo, base, wsq, PSTR);
        asm volatile("s_waitcnt vmcnt(0)" : "+v"(pv[0]), "+v"(pv[1]), "+v"(pv[2]), "+v"(pv[3]), "+v"(pv[4]), "+v"(pv[5]), "+v"(pv[6]), "+v"(pv[7]),
                     "+v"(pv[8]), "+v"(pv[9]), "+v"(pv[10]), "+v"(pv[11]), "+v"(pv[12]), "+v"(pv[13]), "+v"(pv[14]), "+v"(pv[15]) :: "memory");
        const u32 m = todo;
#pragma unroll
        for (int p = 0; p < 16; p++) {
          if (m & (1u << p)) {
            if (!anysent(mx4(0u, pv[p]))) {
              todo &= ~(1u << p);
#pragma unroll
              for (int e = 0; e < 4; e++) s[e] += (f32x2){bflo(pv[p][e]), bfhi(pv[p][e])};
            }
          }
        }
      }
      STAMP(9);
      if (val) {
        *(LAS u32x2*)(sm + ka) = (u32x2){pk2(s[0][0], s[0][1]), pk2(s[1][0], s[1][1])};
        *(LAS u32x2*)(sm + kb) = (u32x2){pk2(s[2][0], s[2][1]), pk2(s[3][0], s[3][1])};
      }
    }
    BARRIER_LDS();
    STAMP(10);
    o_stage();
    STAMP(11);
    {
      const int tp = v & 1, kh = v >> 1;
      const int ft = 2 * tp + kh;
      const int tok = bi * 128 + th * 64 + ft * 16 + c16;
      u32x2 xr[5];
      const u32x2* xp = (const u32x2*)(x + tok * 640 + co * 80 + 20 * g);
#pragma unroll
      for (int e = 0; e < 5; e++) xr[e] = xp[e];
      const f32x2 st = *(LAS f32x2*)(sm + L_STAT + (th * 64 + ft * 16 + c16) * 8);
      LAS char* wrd = kvrd + kh * 51200;
      const u32 ob = WS_O + ((bi * 8 + 4 * th + 2 * tp) * 80 + 40 * kh) * 256 + l * 16;
      u32x4 of[2][10]; u32 spins = 0;
      f32x4 oc[2][5];
      if (v == 0) { poll16(wsr, WS_FO + bi * 2048, tag, l, tmo); cnt_signal(1); } else cnt_wait(sm, 1, 2);
      STAMP(12);
#pragma unroll
      for (int n = 0; n < 10; n++) {
        of[0][n] = __builtin_amdgcn_raw_buffer_load_b128(wsr, ob, n * 1024, 16);
        of[1][n] = __builtin_amdgcn_raw_buffer_load_b128(wsr, ob + 20480, n * 1024, 16);
      }
      for (;;) {
        u32x4 wf[3][5];
#pragma unroll
        for (int n = 0; n < 2; n++)
#pragma unroll
          for (int T = 0; T < 5; T++) wf[n][T] = trd2(wrd + n * 5120 + 8 * T, 2560);
#pragma unroll
        for (int T = 0; T < 5; T++) { oc[0][T] = (f32x4){0.f, 0.f, 0.f, 0.f}; oc[1][T] = (f32x4){0.f, 0.f, 0.f, 0.f}; }
        u32 m = 0;
        SB();
#pragma unroll
        for (int n = 0; n < 10; n++) {
          if (n + 2 < 10) {
#pragma unroll
            for (int T = 0; T < 5; T++) wf[(n + 2) % 3][T] = trd2(wrd + (n + 2) * 5120 + 8 * T, 2560);
          }
#pragma unroll
          for (int T = 0; T < 5; T++) { oc[0][T] = mfma(wf[n % 3][T], of[0][n], oc[0][T]); oc[1][T] = mfma(wf[n % 3][T], of[1][n], oc[1][T]); }
          m = mx4(mx4(m, of[0][n]), of[1][n]);
#pragma unroll
          for (int i = 0; i < 10; i++) { SGB(0x8, 1); SGB(0x100, 1); }
          SB();
        }
        if (!anysent(m)) break;
        u32 todo = 0;
#pragma unroll
        for (int n = 0; n < 10; n++) todo |= anysent(mx4(mx4(0u, of[0][n]), of[1][n])) ? (1u << n) : 0u;
        while (todo) {
          if (++spins > SPIN_CAP) { tmo = true; break; }
          CL<10>::run(of[0], todo, ob, wsq, 1024);
          CL<10>::run(of[1], todo, ob + 20480, wsq, 1024);
          asm volatile("s_waitcnt vmcnt(0)" : "+v"(of[0][0]), "+v"(of[0][1]), "+v"(of[0][2]), "+v"(of[0][3]), "+v"(of[0][4]), "+v"(of[0][5]), "+v"(of[0][6]), "+v"(of[0][7]), "+v"(of[0][8]), "+v"(of[0][9]),
                       "+v"(of[1][0]), "+v"(of[1][1]), "+v"(of[1][2]), "+v"(of[1][3]), "+v"(of[1][4]), "+v"(of[1][5]), "+v"(of[1][6]), "+v"(of[1][7]), "+v"(of[1][8]), "+v"(of[1][9]) :: "memory");
          const u32 mm = todo;
#pragma unroll
          for (int n = 0; n < 10; n++) if (mm & (1u << n)) { if (!anysent(mx4(mx4(0u, of[0][n]), of[1][n]))) todo &= ~(1u << n); }
          if (todo) wait_until(__builtin_amdgcn_s_memrealtime() + D_BK);
        }
        if (tmo) break;
      }
#pragma unroll
      for (int T = 0; T < 5; T++) *(LAS f32x4*)(sm + L_RB + ((v * 5 + T) * 64 + l) * 16) = kh ? oc[0][T] : oc[1][T];
      cnt_signal(4 + v);
      STAMP(13);
      const float a1 = 1.f + st[1], a0 = -st[0] * st[1];
      f32x4 base4[5];
#pragma unroll
      for (int T = 0; T < 5; T++) base4[T] = (kh ? oc[1][T] : oc[0][T]) + (f32x4){bflo(xr[T].x) * a1 + a0, bfhi(xr[T].x) * a1 + a0, bflo(xr[T].y) * a1 + a0, bfhi(xr[T].y) * a1 + a0};
      cnt_wait(sm, 3, 1);
      cnt_wait(sm, 4 + (v ^ 2), 2);
      STAMP(14);
      const bool sel = *(LAS u32*)(sm + L_Y + 320) == (u32)tok;
      u32x2* op = (u32x2*)(out + tok * 640 + co * 80 + 20 * g);
#pragma unroll
      for (int T = 0; T < 5; T++) {
        f32x4 o2 = base4[T] + *(LAS f32x4*)(sm + L_RB + (((v ^ 2) * 5 + T) * 64 + l) * 16);
        if (sel) o2 += *(LAS f32x4*)(sm + L_Y + (20 * g + 4 * T) * 4);
        op[T] = (u32x2){pk2(o2[0], o2[1]), pk2(o2[2], o2[3])};
      }
    }
  }
  if (tmo && l == 0) *(u32*)(ws + WS_TMO) = 1u;
  STAMP(15);
}
}

extern "C" void kernel_launch(void* const* d_in, const int* in_sizes, int n_in, void* d_out, int out_size, void* d_ws, size_t ws_size, hipStream_t stream) {
  using namespace fk;
  (void)hipFuncSetAttribute((const void*)fused, hipFuncAttributeMaxDynamicSharedMemorySize, L_TOTAL);
  fused<<<256, 512, L_TOTAL, stream>>>((const u16*)d_in[0], (const u16*)d_in[1], (const u16*)d_in[2], (const u16*)d_in[3], (const u16*)d_in[5],
                                       (const u16*)d_in[6], (const u16*)d_in[7], (const u16*)d_in[8], (u16*)d_out, (char*)d_ws);
}
```

```cpp
#include <hip/hip_runtime.h>
typedef unsigned short u16;
typedef unsigned int u32;
typedef unsigned long long u64;
typedef short s16x4 __attribute__((ext_vector_type(4)));
typedef __bf16 bf16x2 __attribute__((ext_vector_type(2)));
typedef __bf16 bf16x8 __attribute__((ext_vector_type(8)));
typedef float f32x2 __attribute__((ext_vector_type(2)));
typedef float f32x4 __attribute__((ext_vector_type(4)));
typedef u32 u32x2 __attribute__((ext_vector_type(2)));
typedef u32 u32x4 __attribute__((ext_vector_type(4)));
#define LAS __attribute__((address_space(3)))
#define GAS __attribute__((address_space(1)))
#define DI __device__ __forceinline__

namespace fk {
constexpr u32 WS_EPOCH = 0;
constexpr u32 WS_FLAG = 4096;
constexpr u32 WS_TMO = 8192;
constexpr u32 WS_CAND = 16384;
constexpr u32 WS_STAT = 32768;
constexpr u32 WS_FK = 65536;
constexpr u32 WS_FO = 98304;
constexpr u32 WS_GU = 131072;
constexpr u32 WS_YP = 622592;
constexpr u32 WS_CSX = 786432;
constexpr u32 WS_PART = 1048576;
constexpr u32 PSTR = 3584;
constexpr u32 WS_O = WS_PART + 1048576;
constexpr u32 WS_END = WS_O + 2621440;
constexpr u32 SLOT = 32768, QO = 10240, XO = 16384;
constexpr u32 L_EX = 131072, L_MISC = 136192, L_TOTAL = 163840;
constexpr u32 L_CS = L_MISC;
constexpr u32 L_ZERO = L_MISC + 1024;
constexpr u32 L_CNT = L_MISC + 1088;
constexpr u32 L_WT = L_MISC + 1152;
constexpr u32 L_STAT = L_MISC + 4992;
constexpr u32 L_Y = L_MISC + 6016;
constexpr u32 L_TAG = L_MISC + 6400;
constexpr u32 L_CNT2 = L_MISC + 6408;
constexpr u32 L_HB = L_MISC + 6944;
constexpr u32 L_KVR = 143360;
constexpr u32 L_HO = 147456;
constexpr u32 L_PK = 102400, L_V = 122880;
constexpr u32 SENT = 0xFFFFFFFFu;
constexpr u32 OOB = 0x7FFF0000u;
constexpr u32 SPIN_CAP = 1u << 17;
constexpr u32 ONES = 0x3F803F80u;
constexpr u32 CODE_BYTES = 36864;
constexpr int P_SLP = 2, D_BK = 30;

DI float bflo(u32 v) { return __uint_as_float(v << 16); }
DI float bfhi(u32 v) { return __uint_as_float(v & 0xffff0000u); }
DI u32 pk2(float a, float b) { f32x2 f = {a, b}; return __builtin_bit_cast(u32, __builtin_convertvector(f, bf16x2)); }
DI float dot2(u32 a, u32 b, float c) { return __builtin_amdgcn_fdot2_f32_bf16(__builtin_bit_cast(bf16x2, a), __builtin_bit_cast(bf16x2, b), c, false); }
DI f32x4 mfma(u32x4 a, u32x4 b, f32x4 c) { return __builtin_amdgcn_mfma_f32_16x16x32_bf16(__builtin_bit_cast(bf16x8, a), __builtin_bit_cast(bf16x8, b), c, 0, 0, 0); }
DI u32x2 trd(LAS char* p) { return __builtin_bit_cast(u32x2, __builtin_amdgcn_ds_read_tr16_b64_v4i16((LAS s16x4*)p)); }
DI u32x4 trd2(LAS char* p, int d) { u32x2 a = trd(p), b = trd(p + d); return (u32x4){a.x, a.y, b.x, b.y}; }
DI float xs16(float v) { auto r = __builtin_amdgcn_permlane16_swap(__float_as_uint(v), __float_as_uint(v), false, false); return __uint_as_float(r[0]) + __uint_as_float(r[1]); }
DI float xs32(float v) { auto r = __builtin_amdgcn_permlane32_swap(__float_as_uint(v), __float_as_uint(v), false, false); return __uint_as_float(r[0]) + __uint_as_float(r[1]); }
DI float xmax16(float v) { auto r = __builtin_amdgcn_permlane16_swap(__float_as_uint(v), __float_as_uint(v), false, false); return fmaxf(__uint_as_float(r[0]), __uint_as_float(r[1])); }
DI float xmin16(float v) { auto r = __builtin_amdgcn_permlane16_swap(__float_as_uint(v), __float_as_uint(v), false, false); return fminf(__uint_as_float(r[0]), __uint_as_float(r[1])); }
template <int CTRL> DI u32 dpp(u32 v) { return __builtin_amdgcn_update_dpp(v, v, CTRL, 0xF, 0xF, false); }
DI u32 umax(u32 a, u32 b) { return a > b ? a : b; }
DI u32 rowmax_u32(u32 k) { k = umax(k, dpp<0x128>(k)); k = umax(k, dpp<0x124>(k)); k = umax(k, dpp<0x122>(k)); k = umax(k, dpp<0x121>(k)); return k; }
DI float xs8(float v) { return v + __uint_as_float(dpp<0x128>(__float_as_uint(v))); }
DI u32 san(u32 v) { return v < 0xFFFFFFFEu ? v : 0xFFFFFFFEu; }
DI u32 mx4(u32 m, u32x4 v) { return umax(umax(umax(m, v.x), v.y), umax(v.z, v.w)); }
DI bool anysent(u32 m) { return __builtin_amdgcn_ballot_w64(m == SENT) != 0; }
DI void dma16(u32 voff, const void* sbase, u32 lds_dst) {
  asm volatile("s_mov_b32 m0, %2\n\ts_nop 0\n\tglobal_load_lds_dwordx4 %0, %1" :: "v"(voff), "s"(sbase), "s"(lds_dst) : "memory");
}
DI void dma16m(u32 voff, const void* sbase, u32 lds_dst, u64 mask) {
  asm volatile("s_mov_b32 m0, %2\n\ts_mov_b64 exec, %3\n\tglobal_load_lds_dwordx4 %0, %1\n\ts_mov_b64 exec, -1" :: "v"(voff), "s"(sbase), "s"(lds_dst), "s"(mask) : "memory");
}
template <int BIT> DI void cload(u32x4& dst, u32 todo, u32 voff, u32x4 rs, u32 soff) {
  asm volatile("s_bitcmp1_b32 %2, %5\n\ts_cbranch_scc0 1f\n\tbuffer_load_dwordx4 %0, %1, %3, %4 offen sc1\n1:"
               : "+v"(dst) : "v"(voff), "s"(todo), "s"(rs), "s"(soff), "i"(BIT) : "scc", "memory");
}
template <int N, int I = 0> struct CL { static DI void run(u32x4 (&d)[N], u32 todo, u32 voff, u32x4 rs, u32 stride) {
  if constexpr (I < N) { cload<I>(d[I], todo, voff, rs, I * stride); CL<N, I + 1>::run(d, todo, voff, rs, stride); } } };
#define SB() __builtin_amdgcn_sched_barrier(0)
#define SGB(m, n) __builtin_amdgcn_sched_group_barrier(m, n, 0)
#define VM0() __builtin_amdgcn_s_waitcnt(0x0F70)
#define PIN(x) asm volatile("" : "+v"(x))
#define BARRIER_LDS() asm volatile("s_waitcnt lgkmcnt(0)\n\ts_barrier" ::: "memory")
#define WAITB(N) asm volatile("s_waitcnt vmcnt(" #N ") lgkmcnt(0)\n\ts_barrier" ::: "memory")

DI void wait_until(u64 t) { while (__builtin_amdgcn_s_memrealtime() < t) __builtin_amdgcn_s_sleep(1); }
DI void cnt_signal(int idx) {
  const u32 a = L_CNT + idx * 4, one = 1u;
  asm volatile("s_waitcnt lgkmcnt(0)\n\ts_mov_b64 exec, 1\n\tds_add_u32 %0, %1\n\ts_mov_b64 exec, -1" :: "v"(a), "v"(one) : "memory");
}
DI bool cnt_last(int idx, u32 n) {
  const u32 a = L_CNT2 + idx * 4, one = 1u; u32 r;
  asm volatile("s_mov_b64 exec, 1\n\tds_add_rtn_u32 %0, %1, %2\n\ts_waitcnt lgkmcnt(0)\n\ts_mov_b64 exec, -1" : "=&v"(r) : "v"(a), "v"(one) : "memory");
  return (u32)__builtin_amdgcn_readfirstlane(r) == n - 1;
}
DI void poll16(const __amdgpu_buffer_rsrc_t wsr, u32 base, u32 tag, int l, bool& tmo) {
  u32 spins = 0;
  for (;;) {
    asm volatile("" ::: "memory");
    const u32 f = __builtin_amdgcn_raw_buffer_load_b32(wsr, l < 16 ? base + l * 128 : OOB, 0, 16);
    if (((u32)__builtin_amdgcn_ballot_w64(f == tag) & 0xFFFFu) == 0xFFFFu) break;
    if (++spins > SPIN_CAP) { tmo = true; break; }
    __builtin_amdgcn_s_sleep(P_SLP);
  }
}
DI void cnt_wait(LAS char* sm, int idx, u32 target) {
  for (;;) {
    const u32 v = __builtin_amdgcn_readfirstlane(__hip_atomic_load((LAS u32*)(sm + L_CNT + idx * 4), __ATOMIC_RELAXED, __HIP_MEMORY_SCOPE_WORKGROUP));
    if (v >= target) break;
  }
  asm volatile("" ::: "memory");
}

DI void dual_softmax(const f32x4 (&p)[3], float rs, float os, u32 (&o)[10]) {
  const float sc = rs * 1.4426950408889634f;
  float mx = p[0][0], mn = p[0][0];
#pragma unroll
  for (int e = 1; e < 10; e++) { mx = fmaxf(mx, p[e >> 2][e & 3]); mn = fminf(mn, p[e >> 2][e & 3]); }
  mx = xmax16(mx) * sc; mn = xmin16(mn) * sc;
  float ep[10], en[10], sp = 0.f, sn = 0.f;
#pragma unroll
  for (int e = 0; e < 10; e++) {
    ep[e] = __builtin_amdgcn_exp2f(__builtin_fmaf(p[e >> 2][e & 3], sc, -mx)); en[e] = __builtin_amdgcn_exp2f(__builtin_fmaf(p[e >> 2][e & 3], -sc, mn));
    sp += ep[e]; sn += en[e];
  }
  sp = __builtin_amdgcn_rcpf(xs16(sp)) * os; sn = __builtin_amdgcn_rcpf(xs16(sn)) * os;
#pragma unroll
  for (int e = 0; e < 5; e++) { o[e] = pk2(ep[2 * e] * sp, ep[2 * e + 1] * sp); o[5 + e] = pk2(en[2 * e] * sn, en[2 * e + 1] * sn); }
}

struct Cand { u32 idx; float r, mean, rstd; };
DI Cand poll_cand(const __amdgpu_buffer_rsrc_t wsr, u32x4 a, u32x4 b, u32 tag, int l, bool& tmo) {
  u32 spins = 0;
  while (!__all(((a.y ^ tag) | (a.w ^ tag) | (b.y ^ tag) | (b.w ^ tag)) == 0)) {
    asm volatile("" ::: "memory");
    if (++spins > SPIN_CAP) { tmo = true; break; }
    __builtin_amdgcn_s_sleep(8);
    a = __builtin_amdgcn_raw_buffer_load_b128(wsr, WS_CAND + l * 32, 0, 16);
    b = __builtin_amdgcn_raw_buffer_load_b128(wsr, WS_CAND + l * 32 + 16, 0, 16);
    VM0();
  }
  u32 bk = a.x, be = 0;
  if ((a.z >> 3) > (bk >> 3)) { bk = a.z; be = 1; }
  if ((b.x >> 3) > (bk >> 3)) { bk = b.x; be = 2; }
  if ((b.z >> 3) > (bk >> 3)) { bk = b.z; be = 3; }
  u32 mk = rowmax_u32(bk >> 3);
  { auto r = __builtin_amdgcn_permlane16_swap(mk, mk, false, false); mk = umax(r[0], r[1]); }
  { auto r = __builtin_amdgcn_permlane32_swap(mk, mk, false, false); mk = umax(r[0], r[1]); }
  const int src = __builtin_ctzll(__ballot((bk >> 3) == mk) | (1ull << 63));
  const u32 ent = (4 * src + __builtin_amdgcn_readlane(be, src)) & 255u;
  const u32 key = __builtin_amdgcn_readlane(bk, src);
  Cand c;
  c.idx = ent * 8 + (7u - (key & 7u));
  const u32 kb = key | 4u;
  c.r = __uint_as_float((kb & 0x80000000u) ? (kb & 0x7FFFFFFFu) : ~kb);
  c.mean = 0.f; c.rstd = 0.f;
  return c;
}
DI void check_flags(const __amdgpu_buffer_rsrc_t wsr, u32 tag, int l, bool& tmo) {
  u32 spins = 0;
  for (;;) {
    asm volatile("" ::: "memory");
    const u32x4 fl = __builtin_amdgcn_raw_buffer_load_b128(wsr, WS_FLAG + l * 16, 0, 16);
    if (__all(fl.x == tag && fl.y == tag && fl.z == tag && fl.w == tag)) break;
    if (++spins > SPIN_CAP) { tmo = true; break; }
    __builtin_amdgcn_s_sleep(1);
  }
}

__global__ __launch_bounds__(512, 2) void fused(const u16* __restrict__ x, const u16* __restrict__ wqkv, const u16* __restrict__ wphi,
                                                const u16* __restrict__ wo, const u16* __restrict__ wr, const u16* __restrict__ wg,
                                                const u16* __restrict__ wu, const u16* __restrict__ wd, u16* __restrict__ out, char* __restrict__ ws) {
  extern __shared__ __attribute__((aligned(16))) char smem_[];
  LAS char* sm = (LAS char*)smem_;
  const int tid = threadIdx.x, l = tid & 63, c16 = l & 15, g = l >> 4;
  const int w = __builtin_amdgcn_readfirstlane(tid >> 6);
  const int v = w & 3;
  const int bid = blockIdx.x, xcd = bid & 7, slot = bid >> 3;
  const int bi = 4 * (xcd >> 1) + (slot & 3), bj = 8 * (xcd & 1) + (slot >> 2);
  const int r0 = (((slot >> 2) * 5 / 2 + (slot & 3) * 5) >> 1) % 10;
  const int qj = bj ^ 1, bq = bj >> 1;
  const int q4 = c16 >> 2, p4 = c16 & 3;
  const int th = bj & 1, co = bj >> 1;
  const int bb = bi * 16 + bj;
  const int fs = bb % 24, ks = bb / 24;
  const bool bon = bb < 240;
  const int tile = 2 * v + (w >> 2);
  const __amdgpu_buffer_rsrc_t wsr = __builtin_amdgcn_make_buffer_rsrc(ws, 0, WS_END, 0x00020000);
  bool tmo = false;
  const u32x4 wsq = {(u32)(u64)ws, (u32)((u64)ws >> 32), WS_END, 0x00020000u};
  LAS char* kvrd = sm + (4 * g + q4) * 160 + p4 * 40;
  LAS char* qrd = sm + QO + (4 * g + q4) * 96 + p4 * 8;
  LAS char* xrd0 = sm + XO + v * 4096 + (c16 >> 3) * 1024 + (c16 & 7) * 128 + ((g ^ (c16 >> 1)) * 16);
  LAS char* xrd1 = sm + XO + v * 4096 + (c16 >> 3) * 1024 + (c16 & 7) * 128 + (((4 + g) ^ (c16 >> 1)) * 16);
  u32 tag = 0;
  u32 phq[10];
  u32x4 a2k[3][3];
  u32 a2ad[3], oad[3];
  auto addr_pre = [&]() {
    const int hr = q4 >> 1;
#pragma unroll
    for (int t = 0; t < 3; t++) {
      const int fl_ = 4 * t + p4; const bool fv = fl_ < 10 && g == hr; const int f = 10 * (q4 & 1) + fl_;
      a2ad[t] = fv ? L_WT + ((2 * (bj & 1) + hr) * 20 + f) * 48 : L_ZERO;
      PIN(a2ad[t]);
      int row;
      if (t < 2) { const int c = 8 * q4 + 4 * t + p4; row = c + (c >= 20); }
      else row = p4 < 2 ? 33 + 2 * q4 + p4 : (p4 == 2 ? 20 : 41);
      oad[t] = (row >= 21) == (g >> 1) ? L_KVR + row * 96 + (g & 1) * 48 : L_ZERO;
      PIN(oad[t]);
    }
  };
  auto load_a2k = [&]() {
#pragma unroll
    for (int t = 0; t < 3; t++)
#pragma unroll
      for (int s2 = 0; s2 < 3; s2++) a2k[t][s2] = *(LAS u32x4*)(sm + a2ad[t] + 16 * s2);
  };
  auto phik_mma = [&](f32x4 (&pk)[3], u32x4 b0, u32x4 b1, u32x4 b2) {
#pragma unroll
    for (int t = 0; t < 3; t++) pk[t] = mfma(a2k[t][0], b0, (f32x4){0.f, 0.f, 0.f, 0.f});
#pragma unroll
    for (int t = 0; t < 3; t++) pk[t] = mfma(a2k[t][1], b1, pk[t]);
#pragma unroll
    for (int t = 0; t < 3; t++) pk[t] = mfma(a2k[t][2], b2, pk[t]);
  };
  auto phik_fin = [&](const f32x4 (&pk)[3], float rs) {
    u32 ph[10];
    dual_softmax(pk, rs, rs, ph);
    LAS u32x2* pp = (LAS u32x2*)(sm + L_PK + (tile * 16 + c16) * 160 + g * 40);
#pragma unroll
    for (int e = 0; e < 5; e++) pp[e] = (u32x2){ph[2 * e], ph[2 * e + 1]};
  };
  auto phiq_tile = [&](u32x4 b0, u32x2 qa, float rs) {
    u32x4 a0[3]; u32x2 a1[3];
    const int hr = q4 >> 1;
#pragma unroll
    for (int t = 0; t < 3; t++) {
      const int fl_ = 4 * t + p4; const bool fv = fl_ < 10; const int f = 10 * (q4 & 1) + fl_;
      const u32 rowb = L_WT + ((2 * (qj & 1) + hr) * 20 + f) * 48;
      const u32 alo = (fv && hr == 0) ? rowb + 8 * g : L_ZERO;
      const u32 ahi = (fv && ((g == 0) == (hr == 0))) ? rowb + (g == 0 ? 32 : 8 * (g - 1)) : L_ZERO;
      const u32 aa = (fv && hr == 1 && g < 2) ? rowb + 24 + 8 * g : L_ZERO;
      const u32x2 lo = *(LAS u32x2*)(sm + alo), hi = *(LAS u32x2*)(sm + ahi);
      a0[t] = (u32x4){lo.x, lo.y, hi.x, hi.y}; a1[t] = *(LAS u32x2*)(sm + aa);
    }
    SB();
    f32x4 pq[3];
#pragma unroll
    for (int t = 0; t < 3; t++) pq[t] = mfma(a0[t], b0, (f32x4){0.f, 0.f, 0.f, 0.f});
#pragma unroll
    for (int t = 0; t < 3; t++) pq[t] = mfma((u32x4){a1[t].x, a1[t].y, 0u, 0u}, (u32x4){qa.x, qa.y, 0u, 0u}, pq[t]);
    dual_softmax(pq, rs, 1.f, phq);
  };
  auto o_stage = [&]() {
    u32x4 A[3][3];
#pragma unroll
    for (int u = 0; u < 3; u++)
#pragma unroll
      for (int s3 = 0; s3 < 3; s3++) A[u][s3] = *(LAS u32x4*)(sm + oad[u] + 16 * s3);
    SB();
    f32x4 od[3];
#pragma unroll
    for (int u = 2; u >= 0; u--) od[u] = mfma(A[u][0], (u32x4){phq[0], phq[1], phq[2], phq[3]}, (f32x4){0.f, 0.f, 0.f, 0.f});
#pragma unroll
    for (int u = 2; u >= 0; u--) od[u] = mfma(A[u][1], (u32x4){phq[4], phq[5], phq[6], phq[7]}, od[u]);
#pragma unroll
    for (int u = 2; u >= 0; u--) od[u] = mfma(A[u][2], (u32x4){phq[8], phq[9], 0u, 0u}, od[u]);
    const float ia = __builtin_amdgcn_rcpf(od[2][2] + 1e-6f), ib = __builtin_amdgcn_rcpf(od[2][3] + 1e-6f);
    const float i0 = g <= 2 ? ia : ib, i1 = g <= 1 ? ia : ib;
    const u32 ob = WS_O + ((bi * 8 + tile) * 80 + 5 * qj) * 256;
    __builtin_amdgcn_raw_buffer_store_b128((u32x4){san(pk2(od[0][0] * i0, od[0][1] * i0)), san(pk2(od[0][2] * i0, od[0][3] * i0)), san(pk2(od[1][0] * i1, od[1][1] * i1)), san(pk2(od[1][2] * i1, od[1][3] * i1))}, wsr, ob + l * 16, 0, 16);
    __builtin_amdgcn_raw_buffer_store_b32(san(pk2(od[2][0] * ib, od[2][1] * ib)), wsr, ob + 1024 + c16 * 16 + 4 * g, 0, 16);
    if (cnt_last(1, 8) && l == 0) __builtin_amdgcn_raw_buffer_store_b32(tag, wsr, WS_FO + (bi * 16 + qj) * 128, 0, 16);
  };

  if (w >= 4) {
    Cand cd = {0u, 0.f, 0.f, 0.f};
    u32x4 ka = {0u, 0u, 0u, 0u}, kb_ = ka; u32 xq[4] = {0u, 0u, 0u, 0u};
    u32x4 bw[8], bd[8]; u32 pfw = 0;
#pragma unroll
    for (int n = 0; n < 8; n++) { bw[n] = (u32x4){0u, 0u, 0u, 0u}; bd[n] = bw[n]; }
    if (v < 2) {
      __builtin_amdgcn_s_setprio(3);
      const int r8 = l >> 3;
      const u32 xoff = (bi * 128 + v * 64 + r8) * 1280 + (((l & 7) ^ (r8 >> 1)) * 16);
      const u32 xoff2 = (xoff ^ 64u) + 8 * 1280;
      auto rotd = [&](int c) { int d = c + r0; return d >= 10 ? d - 10 : d; };
      auto issue_x = [&](int c) {
        const char* b = (const char*)x + rotd(c) * 128;
        const u32 dst = (c & 3) * SLOT + XO + v * 8192;
#pragma unroll
        for (int n = 0; n < 4; n++) { dma16(xoff, b + n * 20480, dst + n * 2048); dma16(xoff2, b + n * 20480, dst + n * 2048 + 1024); }
      };
      issue_x(0);
      u32 wvoff[3], wqoff[2]; u64 qm[2];
#pragma unroll
      for (int u = 0; u < 3; u++) {
        const int c = 64 * u + (u == 2 ? (l & 31) : l), rl = c / 10, cc = c % 10;
        wvoff[u] = (v * 32 + 8 * (rl >> 2) + (rl & 3)) * 3840 + (cc < 5 ? 1280 + 16 * cc : 2560 + 16 * (cc - 5)) + 80 * bj;
      }
#pragma unroll
      for (int u = 0; u < 2; u++) {
        const int c = 64 * u + (u == 1 ? (l & 31) : l), rl = c / 6, cc = c % 6;
        wqoff[u] = (v * 32 + 8 * (rl >> 2) + (rl & 3)) * 3840 + 80 * qj + 16 * cc;
        qm[u] = __ballot(cc < 5 && (u == 0 || l < 32));
      }
      auto issue_w = [&](int c) {
#pragma unroll
        for (int hq = 0; hq < 2; hq++) {
          const char* wb = (const char*)wqkv + rotd(c) * 245760 + hq * 15360;
          const u32 sb = (c & 3) * SLOT + (2 * v + hq) * 2560, sq_ = (c & 3) * SLOT + QO + (2 * v + hq) * 1536;
          dma16(wvoff[0], wb, sb); dma16(wvoff[1], wb, sb + 1024); dma16m(wvoff[2], wb, sb + 2048, 0xFFFFFFFFull);
          dma16m(wqoff[0], wb, sq_, qm[0]); dma16m(wqoff[1], wb, sq_ + 1024, qm[1]);
        }
      };
      issue_w(0);
#pragma unroll 1
      for (int n = 1; n < 10; n++) {
        if (n == 2) WAITB(18); else if (n >= 4) WAITB(36);
        issue_x(n); issue_w(n);
      }
      WAITB(36);
      WAITB(0);
      __builtin_amdgcn_s_setprio(0);
      {
        const int lc = v * 50 + (l < 50 ? l : 0);
        const u32 po = (slot * 20 + lc / 5) * 1280 + (xcd & 1) * 640 + (lc % 5) * 128;
        asm volatile("global_load_dword %0, %1, %2" : "=v"(pfw) : "v"(po), "s"(wo) : "memory");
        const u32x4 sv = {SENT, SENT, SENT, SENT};
        const int ht = tid - 256;
#pragma unroll
        for (int n = 0; n < 2; n++) { const int e = ht + 128 * n; __builtin_amdgcn_raw_buffer_store_b128(sv, wsr, e < 224 ? WS_PART + (bj * 16 + bi) * PSTR + e * 16 : OOB, 0, 16); }
#pragma unroll
        for (int n = 0; n < 5; n++) { const int e = ht + 128 * n; __builtin_amdgcn_raw_buffer_store_b128(sv, wsr, WS_O + ((bi * 8 + e / 80) * 80 + 5 * qj) * 256 + (e % 80) * 16, 0, 16); }
      }
      addr_pre();
      load_a2k(); tag = __builtin_amdgcn_readfirstlane(*(LAS u32*)(sm + L_TAG));
    } else {
      f32x4 cs4[4];
#pragma unroll
      for (int n = 0; n < 4; n++) cs4[n] = (f32x4){0.f, 0.f, 0.f, 0.f};
      const u32x4 ones4 = {ONES, ONES, ONES, ONES};
      const int qq = bi >> 2, aa = (slot >> 2) * 5 / 2, span = ((aa + 5 * (slot & 3) + 5) >> 1) - ((aa + 5 * (slot & 3)) >> 1);
      auto cs_step = [&](int st) {
        LAS char* pa = kvrd + st * SLOT + (qq & 1) * 5120;
        LAS char* pb = v == 2 ? pa + 8 : qrd + st * SLOT + (qq & 1) * 3072;
        const int ts = v == 2 ? 8 : 32, d1 = v == 2 ? 2560 : 1536;
        const u32x4 f0 = trd2(pa + (v == 2 ? 0 : 32), 2560);
        u32x4 f[3];
#pragma unroll
        for (int n = 0; n < 3; n++) { const u32x2 lo = trd(pb + n * ts), hi = trd(pb + n * ts + d1); f[n] = (u32x4){lo.x, lo.y, hi.x, hi.y}; }
        cs4[0] = mfma(f0, ones4, cs4[0]);
#pragma unroll
        for (int n = 0; n < 3; n++) cs4[1 + n] = mfma(f[n], ones4, cs4[1 + n]);
      };
      addr_pre();
      BARRIER_LDS();
      tag = __builtin_amdgcn_readfirstlane(*(LAS u32*)(sm + L_TAG));
      if (v == 3) {
        const u32x4 e = *(LAS u32x4*)(sm + L_EX + (l & 7) * 16);
        u32 mk = e.x; mk = umax(mk, dpp<0x124>(mk)); mk = umax(mk, dpp<0x122>(mk)); mk = umax(mk, dpp<0x121>(mk));
        if (l < 8) __builtin_amdgcn_raw_buffer_store_b128((u32x4){e.y, tag, e.z, tag}, wsr, WS_STAT + (bb * 8 + l) * 16, 0, 16);
        if (l < 8 && e.x == mk) {
          __builtin_amdgcn_raw_buffer_store_b64((u32x2){e.x, tag}, wsr, WS_CAND + bb * 8, 0, 16);
          __builtin_amdgcn_raw_buffer_store_b32(tag, wsr, WS_FLAG + bid * 4, 0, 16);
        }
      }
#pragma unroll 1
      for (int st = 0; st < 3; st++) {
        if (st > 0) BARRIER_LDS();
        if (st == (qq >> 1) || (st == 2 && qq < 2 && span == 3)) cs_step(st);
      }
      {
        const int n = c16 >> 1, hf = c16 & 1;
        const f32x4 cv = n == 0 ? cs4[0] : (n == 1 ? cs4[1] : (n == 2 ? cs4[2] : cs4[3]));
        if (c16 < 8)
          __builtin_amdgcn_raw_buffer_store_b128((u32x4){__float_as_uint(hf ? cv[2] : cv[0]), tag, __float_as_uint(hf ? cv[3] : cv[1]), tag}, wsr,
                                                 WS_CSX + (bj * 16 + bi) * 1024 + (((v - 2) * 4 + n) * 16 + 4 * g + 2 * hf) * 8, 0, 16);
      }
      load_a2k();
      BARRIER_LDS();
      u32x4 sv_;
      asm volatile("buffer_load_dwordx4 %0, %3, %4, 0 offen sc1\n\tbuffer_load_dwordx4 %1, %3, %4, 0 offen offset:16 sc1\n\tbuffer_load_dwordx4 %2, %5, %4, 0 offen sc1"
                   : "=&v"(ka), "=&v"(kb_), "=&v"(sv_) : "v"((u32)(WS_CAND + l * 32)), "s"(wsq), "v"((u32)(WS_STAT + (bi * 128 + (v - 2) * 64 + l) * 16)) : "memory");
      {
        const u32 bo = bon ? ((ks * 64 + (v - 2) * 32 + (l >> 3)) * 1536 + fs * 64 + (l & 7) * 8) * 2 : l * 16;
#pragma unroll
        for (int n = 0; n < 4; n++) asm volatile("global_load_dwordx4 %0, %1, %2" : "=v"(bw[n]) : "v"(bo + n * 24576), "s"(wg) : "memory");
        BARRIER_LDS();
#pragma unroll
        for (int n = 0; n < 4; n++) asm volatile("global_load_dwordx4 %0, %1, %2" : "=v"(bw[4 + n]) : "v"(bo + n * 24576), "s"(wu) : "memory");
      }
      BARRIER_LDS();
      asm volatile("s_waitcnt vmcnt(8)" : "+v"(ka), "+v"(kb_), "+v"(sv_) :: "memory");
      { u32 spins = 0;
        while (!__all(sv_.y == tag && sv_.w == tag)) {
          asm volatile("" ::: "memory");
          if (++spins > SPIN_CAP) { tmo = true; break; }
          __builtin_amdgcn_s_sleep(4);
          sv_ = __builtin_amdgcn_raw_buffer_load_b128(wsr, WS_STAT + (bi * 128 + (v - 2) * 64 + l) * 16, 0, 16);
          VM0();
        } }
      *(LAS u32x2*)(sm + L_STAT + ((v - 2) * 64 + l) * 8) = (u32x2){sv_.x, sv_.z};
      cd = poll_cand(wsr, ka, kb_, tag, l, tmo);
      if (tid == 448) *(LAS u32*)(sm + L_Y + 320) = cd.idx;
      u32x4 cg[8];
      const u32 cb = WS_CSX + (bj * 16 + (v - 2) * 8) * 1024 + l * 16;
      {
        const u32 xo = (cd.idx * 640 + (bon ? ks * 64 : 0) + (v - 2) * 32 + (l >> 3)) * 2;
        asm volatile("global_load_ushort %0, %4, %5\n\tglobal_load_ushort %1, %4, %5 offset:16\n\tglobal_load_ushort %2, %4, %5 offset:32\n\tglobal_load_ushort %3, %4, %5 offset:48"
                     : "=&v"(xq[0]), "=&v"(xq[1]), "=&v"(xq[2]), "=&v"(xq[3]) : "v"(xo), "s"(x) : "memory");
        asm volatile("buffer_load_dwordx4 %0, %1, %2, 0 offen sc1" : "=v"(ka) : "v"((u32)(WS_STAT + cd.idx * 16)), "s"(wsq) : "memory");
      }
      BARRIER_LDS();
#pragma unroll
      for (int p = 0; p < 8; p++) asm volatile("buffer_load_dwordx4 %0, %1, %2, 0 offen sc1" : "=v"(cg[p]) : "v"(cb + p * 1024), "s"(wsq) : "memory");
      BARRIER_LDS();
      asm volatile("s_waitcnt vmcnt(8)" : "+v"(ka), "+v"(xq[0]), "+v"(xq[1]), "+v"(xq[2]), "+v"(xq[3]),
                   "+v"(bw[0]), "+v"(bw[1]), "+v"(bw[2]), "+v"(bw[3]), "+v"(bw[4]), "+v"(bw[5]), "+v"(bw[6]), "+v"(bw[7]) :: "memory");
      float ba[16]; u32 xs4[4];
      {
        u32 spins = 0;
        while (!__all(ka.y == tag && ka.w == tag)) {
          asm volatile("" ::: "memory");
          if (++spins > SPIN_CAP) { tmo = true; break; }
          ka = __builtin_amdgcn_raw_buffer_load_b128(wsr, WS_STAT + cd.idx * 16, 0, 16);
          VM0();
        }
        const float cm = __uint_as_float(ka.x), cr = __uint_as_float(ka.z);
#pragma unroll
        for (int e = 0; e < 16; e++) ba[e] = 0.f;
#pragma unroll
        for (int n = 0; n < 4; n++) {
          xs4[n] = pk2((bflo(xq[n]) - cm) * cr, 0.f);
#pragma unroll
          for (int e = 0; e < 4; e++) { ba[2 * e] = dot2(bw[n][e], xs4[n], ba[2 * e]); ba[2 * e + 1] = dot2(bw[n][e], xs4[n] << 16, ba[2 * e + 1]); }
        }
#pragma unroll
        for (int e = 0; e < 8; e++) PIN(ba[e]);
      }
      BARRIER_LDS();
      {
        asm volatile("s_waitcnt vmcnt(0)" : "+v"(cg[0]), "+v"(cg[1]), "+v"(cg[2]), "+v"(cg[3]), "+v"(cg[4]), "+v"(cg[5]), "+v"(cg[6]), "+v"(cg[7]) :: "memory");
        u32 spins = 0;
        for (;;) {
          u32 m = 0;
#pragma unroll
          for (int p = 0; p < 8; p++) m |= (cg[p].y ^ tag) | (cg[p].w ^ tag);
          if (__builtin_amdgcn_ballot_w64(m != 0) == 0) break;
          asm volatile("" ::: "memory");
          if (++spins > SPIN_CAP) { tmo = true; break; }
          __builtin_amdgcn_s_sleep(4);
#pragma unroll
          for (int p = 0; p < 8; p++) cg[p] = __builtin_amdgcn_raw_buffer_load_b128(wsr, cb, p * 1024, 16);
          VM0();
        }
        float s0 = 0.f, s1 = 0.f;
#pragma unroll
        for (int p = 0; p < 8; p++) { s0 += __uint_as_float(cg[p].x); s1 += __uint_as_float(cg[p].z); }
        *(LAS f32x2*)(sm + L_CS + (v - 2) * 512 + l * 8) = (f32x2){s0, s1};
      }
      cnt_signal(1);
      {
#pragma unroll
        for (int n = 0; n < 4; n++)
#pragma unroll
          for (int e = 0; e < 4; e++) { ba[8 + 2 * e] = dot2(bw[4 + n][e], xs4[n], ba[8 + 2 * e]); ba[9 + 2 * e] = dot2(bw[4 + n][e], xs4[n] << 16, ba[9 + 2 * e]); }
        float h8[8], h4[4];
#pragma unroll
        for (int e = 0; e < 8; e++) { auto r = __builtin_amdgcn_permlane32_swap(__float_as_uint(ba[e]), __float_as_uint(ba[8 + e]), false, false); h8[e] = __uint_as_float(r[0]) + __uint_as_float(r[1]); }
#pragma unroll
        for (int e = 0; e < 4; e++) { auto r = __builtin_amdgcn_permlane16_swap(__float_as_uint(h8[e]), __float_as_uint(h8[4 + e]), false, false); h4[e] = xs8(__uint_as_float(r[0]) + __uint_as_float(r[1])); }
        const int hh = (l >> 3) & 1;
        __builtin_amdgcn_raw_buffer_store_b128((u32x4){__float_as_uint(hh ? h4[2] : h4[0]), tag, __float_as_uint(hh ? h4[3] : h4[1]), tag}, wsr,
                                               bon ? WS_GU + (fs * 20 + ks * 2 + v - 2) * 1024 + (l >> 5) * 512 + (l & 7) * 64 + (g & 1) * 32 + hh * 16 : OOB, 0, 16);
      }
      if (v == 2) {
        const u32 bdo = bon ? ((fs * 64 + (l >> 3)) * 640 + ks * 64 + (l & 7) * 8) * 2 : l * 16;
#pragma unroll
        for (int n = 0; n < 8; n++) asm volatile("global_load_dwordx4 %0, %1, %2" : "=v"(bd[n]) : "v"(bdo + n * 10240), "s"(wd) : "memory");
      }
    }
    const float rs1 = *(LAS float*)(sm + L_STAT + ((2 * v + 1) * 16 + c16) * 8 + 4);
    cnt_wait(sm, 8 + v, 1);
    {
      const u32x4 b0 = *(LAS u32x4*)(sm + L_HO + v * 4096 + l * 16), b1 = *(LAS u32x4*)(sm + L_HO + v * 4096 + 1024 + l * 16);
      const u32x2 b2 = *(LAS u32x2*)(sm + L_HO + v * 4096 + 2048 + l * 8);
      f32x4 pk[3];
      phik_mma(pk, b0, b1, (u32x4){b2.x, b2.y, 0u, 0u});
      phik_fin(pk, rs1);
      if (v < 2) asm volatile("s_waitcnt vmcnt(0)" ::: "memory");
      cnt_signal(0);
    }
    asm volatile("s_waitcnt vmcnt(0)" : "+v"(bd[0]), "+v"(bd[1]), "+v"(bd[2]), "+v"(bd[3]), "+v"(bd[4]), "+v"(bd[5]), "+v"(bd[6]), "+v"(bd[7]), "+v"(pfw) :: "memory");
    u32 wooff[3];
#pragma unroll
    for (int u = 0; u < 3; u++) {
      const int c = 64 * u + (u == 2 ? (l & 31) : l), rl = c / 10, cc = c % 10;
      wooff[u] = ((v & 1) * 32 + 8 * (rl >> 2) + (rl & 3)) * 1280 + co * 160 + 16 * cc;
    }
    auto wo_units = [&]() {
      for (int un = 0; un < 20; un++) {
        const char* wb = (const char*)wo + (un >> 1) * 81920 + (un & 1) * 5120;
        const u32 dst = (un >> 1) * 10240 + (v * 2 + (un & 1)) * 2560;
        dma16(wooff[0], wb, dst); dma16(wooff[1], wb, dst + 1024); dma16m(wooff[2], wb, dst + 2048, 0xFFFFFFFFull);
        asm volatile("s_waitcnt vmcnt(30)" ::: "memory");
      }
    };
    bool b3todo = v == 2 && bon, b3hint = false;
    auto b3_try = [&]() {
      const u32 base = WS_GU + fs * 20 * 1024 + l * 16;
      u32x4 gv[20]; u32 m = 0;
      asm volatile("" ::: "memory");
      if (!b3hint) {
        const u32 hv = __builtin_amdgcn_raw_buffer_load_b32(wsr, l < 20 ? WS_GU + (fs * 20 + l) * 1024 + 4 : OOB, 0, 16);
        if (((u32)__builtin_amdgcn_ballot_w64(hv == tag) & 0xFFFFFu) != 0xFFFFFu) return;
      }
#pragma unroll
      for (int p = 0; p < 20; p++) { gv[p] = __builtin_amdgcn_raw_buffer_load_b128(wsr, base, p * 1024, 16); m |= (gv[p].y ^ tag) | (gv[p].w ^ tag); }
      if (__builtin_amdgcn_ballot_w64(m != 0) != 0) return;
      b3todo = false;
      float s0 = 0.f, s1 = 0.f;
#pragma unroll
      for (int p = 0; p < 20; p++) { s0 += __uint_as_float(gv[p].x); s1 += __uint_as_float(gv[p].z); }
      {
        auto r0_ = __builtin_amdgcn_permlane32_swap(__float_as_uint(s0), __float_as_uint(s0), false, false);
        auto r1_ = __builtin_amdgcn_permlane32_swap(__float_as_uint(s1), __float_as_uint(s1), false, false);
        const float g0 = __uint_as_float(r0_[0]), u0 = __uint_as_float(r0_[1]), g1 = __uint_as_float(r1_[0]), u1 = __uint_as_float(r1_[1]);
        const u32 hp = pk2(g0 * __builtin_amdgcn_rcpf(1.f + __expf(-g0)) * u0, g1 * __builtin_amdgcn_rcpf(1.f + __expf(-g1)) * u1);
        if (l < 32) *(LAS u32*)(sm + L_HB + l * 4) = hp;
      }
      asm volatile("s_waitcnt lgkmcnt(0)" ::: "memory");
      float y[8];
#pragma unroll
      for (int e = 0; e < 8; e++) y[e] = 0.f;
#pragma unroll
      for (int n = 0; n < 8; n++) {
        const u32 hs = *(LAS u16*)(sm + L_HB + ((l >> 3) + 8 * n) * 2), hh2 = hs << 16;
#pragma unroll
        for (int e = 0; e < 4; e++) { y[2 * e] = dot2(bd[n][e], hs, y[2 * e]); y[2 * e + 1] = dot2(bd[n][e], hh2, y[2 * e + 1]); }
      }
      float y4[4];
#pragma unroll
      for (int e = 0; e < 4; e++) { auto r = __builtin_amdgcn_permlane32_swap(__float_as_uint(y[e]), __float_as_uint(y[4 + e]), false, false); y4[e] = xs16(xs8(__uint_as_float(r[0]) + __uint_as_float(r[1]))); }
      if ((l & 16) == 0) { const int hh = (l >> 3) & 1;
        __builtin_amdgcn_raw_buffer_store_b128((u32x4){__float_as_uint(hh ? y4[2] : y4[0]), tag, __float_as_uint(hh ? y4[3] : y4[1]), tag}, wsr,
                                               WS_YP + fs * 5120 + ks * 512 + (l & 7) * 64 + (l >> 5) * 32 + hh * 16, 0, 16); }
    };
    auto do_phiq = [&]() {
      cnt_wait(sm, 12 + v, 1);
      phiq_tile(*(LAS u32x4*)(sm + L_HO + v * 4096 + l * 16), *(LAS u32x2*)(sm + L_HO + v * 4096 + 1024 + l * 8), rs1);
    };
    if (b3todo) { b3hint = true; b3_try(); b3hint = false; }
    do_phiq();
    if (v < 2) wo_units();
    BARRIER_LDS();
    o_stage();
    if (v < 2) { asm volatile("s_waitcnt vmcnt(0)" ::: "memory"); cnt_signal(2); }
    { u32 spins = 0; b3hint = true; while (b3todo) { b3_try(); b3hint = false; if (++spins > SPIN_CAP) { tmo = true; break; } __builtin_amdgcn_s_sleep(4); } }
    if (v == 3) {
      const bool mine = (int)(cd.idx >> 6) == bi * 2 + th;
      if (mine) {
        const u32 base = l < 40 ? WS_YP + (co * 80 + l * 2) * 8 : OOB;
        u32x4 vv[24]; u32 sp2 = 0;
        for (;;) {
          asm volatile("" ::: "memory");
          u32 m = 0;
#pragma unroll
          for (int p = 0; p < 24; p++) { vv[p] = __builtin_amdgcn_raw_buffer_load_b128(wsr, base, p * 5120, 16); m |= (vv[p].y ^ tag) | (vv[p].w ^ tag); }
          if (__builtin_amdgcn_ballot_w64(m != 0 && l < 40) == 0) break;
          if (++sp2 > SPIN_CAP) { tmo = true; break; }
          __builtin_amdgcn_s_sleep(4);
        }
        float s0 = 0.f, s1 = 0.f;
#pragma unroll
        for (int p = 0; p < 24; p++) { s0 += __uint_as_float(vv[p].x); s1 += __uint_as_float(vv[p].z); }
        const float sg = __builtin_amdgcn_rcpf(1.f + __expf(-cd.r));
        if (l < 40) *(LAS f32x2*)(sm + L_Y + l * 8) = (f32x2){s0 * sg, s1 * sg};
      }
      cnt_signal(3);
      if (bid == 0) {
        check_flags(wsr, tag, l, tmo);
        if (l == 0) __hip_atomic_store((GAS u32*)(ws + WS_EPOCH), tag, __ATOMIC_RELAXED, __HIP_MEMORY_SCOPE_AGENT);
      }
    }
  } else {
    u32 pfx = 0;
    if (v == 3) {
      u64 pc;
      asm volatile("s_getpc_b64 %0" : "=s"(pc));
      pc = pc & ~(u64)127;
      const u32 o = (slot * 16 + c16) * 128;
      pfx = *(const GAS u32*)(pc + (o < CODE_BYTES ? o : 0u));
    }
    {
      u32x4 xe[3], we[3];
#pragma unroll
      for (int n = 0; n < 3; n++) {
        const int ch = (l & 31) + 32 * n, cc = ch < 80 ? ch : 0;
        xe[n] = *(const u32x4*)(x + (bb * 8 + 2 * v + (l >> 5)) * 640 + cc * 8);
        we[n] = *(const u32x4*)(wr + cc * 8);
      }
      const u32x4 wpc = *(const u32x4*)(wphi + 1600 * bq + (tid < 200 ? tid : 0) * 8);
      const u32 ep_raw = __hip_atomic_load((GAS u32*)(ws + WS_EPOCH), __ATOMIC_RELAXED, __HIP_MEMORY_SCOPE_AGENT);
      if (tid < 32) *(LAS u32*)(sm + L_ZERO + tid * 4) = 0u;
      *(LAS u32x4*)(sm + (tid >> 6) * SLOT + QO + (tid & 63) * 96 + 80) = (u32x4){0u, 0u, 0u, 0u};
      if (tid < 80) *(LAS u32x2*)(sm + L_WT + tid * 48 + 40) = (u32x2){0u, 0u};
      if (tid < 84) *(LAS u32x2*)(sm + L_KVR + tid * 48 + 40) = (u32x2){0u, 0u};
      addr_pre();
      {
        if ((l & 31) >= 16) { xe[2] = (u32x4){0u, 0u, 0u, 0u}; we[2] = xe[2]; }
        float sa[4] = {0.f, 0.f, 0.f, 0.f};
#pragma unroll
        for (int n = 0; n < 3; n++)
#pragma unroll
          for (int m = 0; m < 4; m++) { sa[0] = dot2(xe[n][m], ONES, sa[0]); sa[1] = dot2(xe[n][m], xe[n][m], sa[1]); sa[2] = dot2(xe[n][m], we[n][m], sa[2]); sa[3] = dot2(we[n][m], ONES, sa[3]); }
#pragma unroll
        for (int q = 0; q < 4; q++) {
          sa[q] += __uint_as_float(dpp<0x128>(__float_as_uint(sa[q]))); sa[q] += __uint_as_float(dpp<0x124>(__float_as_uint(sa[q])));
          sa[q] += __uint_as_float(dpp<0x122>(__float_as_uint(sa[q]))); sa[q] += __uint_as_float(dpp<0x121>(__float_as_uint(sa[q])));
          sa[q] = xs16(sa[q]);
        }
        const float mm = sa[0] * (1.f / 640.f);
        const float rs = __builtin_amdgcn_rsqf(fmaxf(sa[1] * (1.f / 640.f) - mm * mm, 0.f) + 1e-5f);
        const u32 rb = __float_as_uint(rs * (sa[2] - mm * sa[3]));
        const int t8 = 2 * v + (l >> 5);
        const u32 key = (((rb & 0x80000000u) ? ~rb : (rb | 0x80000000u)) & ~7u) | (u32)(7 - t8);
        if ((l & 31) == 0) *(LAS u32x4*)(sm + L_EX + t8 * 16) = (u32x4){key, __float_as_uint(mm), __float_as_uint(rs), 0u};
      }
      if (tid < 200) {
#pragma unroll
        for (int j = 0; j < 8; j++) {
          const int e = tid * 8 + j, h = e / 400, r = e % 400, d = r / 20, f = r % 20;
          *(LAS u16*)(sm + L_WT + (h * 20 + f) * 48 + d * 2) = (u16)((j & 1) ? (wpc[j >> 1] >> 16) : (wpc[j >> 1] & 0xFFFFu));
        }
      }
      tag = __builtin_amdgcn_readfirstlane(ep_raw) + 1u;
      if (tid == 0) { *(LAS u32*)(sm + L_TAG) = tag; *(LAS u32x2*)(sm + L_CNT2) = (u32x2){0u, 0u}; *(LAS u32x2*)(sm + L_CNT2 + 8) = (u32x2){0u, 0u}; }
    }
    asm volatile("" :: "v"(pfx));
    f32x4 acc[2][8];
#pragma unroll
    for (int T = 0; T < 8; T++) { acc[0][T] = (f32x4){0.f, 0.f, 0.f, 0.f}; acc[1][T] = (f32x4){0.f, 0.f, 0.f, 0.f}; }
    u32x4 fb[8], xb[2];
#pragma unroll
    for (int T = 0; T < 8; T++) fb[T] = (u32x4){0u, 0u, 0u, 0u};
    xb[0] = fb[0]; xb[1] = fb[0];
    __builtin_amdgcn_s_waitcnt(0xC07F);
    __builtin_amdgcn_s_setprio(1);
#pragma unroll 1
    for (int c = 0; c < 10; c++) {
      if (c < 9) BARRIER_LDS();
      const u32 sb = (c & 3) * SLOT;
      LAS char* kp = kvrd + sb; LAS char* qp = qrd + sb; LAS char* x0 = xrd0 + sb; LAS char* x1 = xrd1 + sb;
      u32x4 fa[8], xa[2];
#pragma unroll
      for (int t = 0; t < 2; t++) xa[t] = *(LAS u32x4*)(x0 + t * 2048);
#pragma unroll
      for (int T = 0; T < 5; T++) fa[T] = trd2(kp + 8 * T, 2560);
#pragma unroll
      for (int T = 0; T < 3; T++) fa[5 + T] = trd2(qp + 32 * T, 1536);
#pragma unroll
      for (int T = 0; T < 8; T++) { acc[0][T] = mfma(fb[T], xb[0], acc[0][T]); acc[1][T] = mfma(fb[T], xb[1], acc[1][T]); }
#pragma unroll
      for (int i = 0; i < 7; i++) { SGB(0x8, 1); SGB(0x100, 2); }
#pragma unroll
      for (int i = 0; i < 4; i++) { SGB(0x8, 1); SGB(0x100, 1); }
      SGB(0x8, 5);
      SB();
#pragma unroll
      for (int t = 0; t < 2; t++) xb[t] = *(LAS u32x4*)(x1 + t * 2048);
#pragma unroll
      for (int T = 0; T < 5; T++) fb[T] = trd2(kp + 5120 + 8 * T, 2560);
#pragma unroll
      for (int T = 0; T < 3; T++) fb[5 + T] = trd2(qp + 3072 + 32 * T, 1536);
#pragma unroll
      for (int T = 0; T < 8; T++) { acc[0][T] = mfma(fa[T], xa[0], acc[0][T]); acc[1][T] = mfma(fa[T], xa[1], acc[1][T]); }
#pragma unroll
      for (int i = 0; i < 7; i++) { SGB(0x8, 1); SGB(0x100, 2); }
#pragma unroll
      for (int i = 0; i < 4; i++) { SGB(0x8, 1); SGB(0x100, 1); }
      SGB(0x8, 5);
      SB();
    }
    float mean[2], rs0; u32 sdp[2]; f32x4 cs[5];
    {
      __builtin_amdgcn_s_setprio(0);
      cnt_wait(sm, 1, 2);
      load_a2k();
      const f32x2 s0 = *(LAS f32x2*)(sm + L_STAT + ((2 * v) * 16 + c16) * 8), s1 = *(LAS f32x2*)(sm + L_STAT + ((2 * v + 1) * 16 + c16) * 8);
#pragma unroll
      for (int T = 0; T < 5; T++) acc[1][T] = mfma(fb[T], xb[1], acc[1][T]);
#pragma unroll
      for (int T = 0; T < 5; T++) acc[0][T] = mfma(fb[T], xb[0], acc[0][T]);
#pragma unroll
      for (int T = 0; T < 5; T++) { cs[T] = *(LAS f32x4*)(sm + L_CS + T * 64 + g * 16) + *(LAS f32x4*)(sm + L_CS + 512 + T * 64 + g * 16); PIN(cs[T]); }
      mean[0] = s0[0]; mean[1] = s1[0]; rs0 = s0[1];
      sdp[0] = pk2(__builtin_amdgcn_rcpf(s0[1]), 0.f); sdp[1] = pk2(__builtin_amdgcn_rcpf(s1[1]), 0.f);
      PIN(sdp[0]); PIN(sdp[1]);
      SB();
    }
    {
      u32 P[2][10];
#pragma unroll
      for (int T = 0; T < 5; T++) {
        const f32x4 vv = acc[1][T] - cs[T] * mean[1];
        P[1][2 * T] = pk2(vv[0], vv[1]); P[1][2 * T + 1] = pk2(vv[2], vv[3]);
      }
      *(LAS u32x4*)(sm + L_HO + v * 4096 + l * 16) = (u32x4){P[1][0], P[1][1], P[1][2], P[1][3]};
      *(LAS u32x4*)(sm + L_HO + v * 4096 + 1024 + l * 16) = (u32x4){P[1][4], P[1][5], P[1][6], P[1][7]};
      *(LAS u32x2*)(sm + L_HO + v * 4096 + 2048 + l * 8) = (u32x2){P[1][8], P[1][9]};
      SB();
#pragma unroll
      for (int T = 0; T < 2; T++) {
        const f32x4 vv = acc[0][T] - cs[T] * mean[0];
        P[0][2 * T] = pk2(vv[0], vv[1]); P[0][2 * T + 1] = pk2(vv[2], vv[3]);
      }
      SB();
      cnt_signal(8 + v);
      SB();
#pragma unroll
      for (int T = 2; T < 5; T++) {
        const f32x4 vv = acc[0][T] - cs[T] * mean[0];
        P[0][2 * T] = pk2(vv[0], vv[1]); P[0][2 * T + 1] = pk2(vv[2], vv[3]);
      }
      f32x4 pk[3];
      phik_mma(pk, (u32x4){P[0][0], P[0][1], P[0][2], P[0][3]}, (u32x4){P[0][4], P[0][5], P[0][6], P[0][7]}, (u32x4){P[0][8], P[0][9], 0u, 0u});
      SB();
      if (g >= 2) {
#pragma unroll
        for (int t = 0; t < 2; t++) {
          LAS u32x4* vp = (LAS u32x4*)(sm + L_V + ((2 * v + t) * 16 + c16) * 96 + (g - 2) * 48);
          vp[0] = (u32x4){P[t][0], P[t][1], P[t][2], P[t][3]}; vp[1] = (u32x4){P[t][4], P[t][5], P[t][6], P[t][7]}; vp[2] = (u32x4){P[t][8], P[t][9], sdp[t], 0u};
        }
      }
      phik_fin(pk, rs0);
    }
    cnt_signal(0);
#pragma unroll
    for (int T = 5; T < 8; T++) { acc[1][T] = mfma(fb[T], xb[1], acc[1][T]); acc[0][T] = mfma(fb[T], xb[0], acc[0][T]); PIN(acc[0][T]); PIN(acc[1][T]); }
    {
      const int h = v >> 1, nt = v & 1;
      u32 apo = L_PK + (4 * g + q4) * 160 + h * 80 + 8 * p4, bpo = L_V + (4 * g + q4) * 96 + h * 48 + 32 * nt + 8 * p4;
      u32 a0 = (nt == 0 || c16 < 5) ? WS_PART + (bj * 16 + bi) * PSTR + (h * 21 + 16 * nt + c16) * 80 + g * 8 : OOB;
      u32 a2 = g < 2 ? a0 + 64 : OOB;
      PIN(apo); PIN(bpo); PIN(a0); PIN(a2);
      cnt_wait(sm, 0, 8);
      LAS char* ap = sm + apo; LAS char* bp = sm + bpo;
      f32x4 kk[3];
#pragma unroll
      for (int m = 0; m < 3; m++) kk[m] = (f32x4){0.f, 0.f, 0.f, 0.f};
      u32x4 B[4], A[4][3];
#pragma unroll
      for (int k4 = 0; k4 < 4; k4++) {
        B[k4] = trd2(bp + k4 * 3072, 1536);
#pragma unroll
        for (int m = 0; m < 3; m++) A[k4][m] = trd2(ap + k4 * 5120 + 32 * m, 2560);
      }
#pragma unroll
      for (int k4 = 0; k4 < 4; k4++)
#pragma unroll
        for (int m = 0; m < 3; m++) kk[m] = mfma(A[k4][m], B[k4], kk[m]);
      SGB(0x100, 20);
#pragma unroll
      for (int i = 0; i < 6; i++) { SGB(0x8, 1); SGB(0x100, 2); }
      SGB(0x8, 6);
      SB();
      __builtin_amdgcn_raw_buffer_store_b64((u32x2){san(pk2(kk[0][0], kk[0][1])), san(pk2(kk[0][2], kk[0][3]))}, wsr, a0, 0, 16);
      __builtin_amdgcn_raw_buffer_store_b64((u32x2){san(pk2(kk[1][0], kk[1][1])), san(pk2(kk[1][2], kk[1][3]))}, wsr, a0 + 32, 0, 16);
      __builtin_amdgcn_raw_buffer_store_b64((u32x2){san(pk2(kk[2][0], kk[2][1])), san(pk2(kk[2][2], kk[2][3]))}, wsr, a2, 0, 16);
    }
    if (cnt_last(0, 4) && l == 0) __builtin_amdgcn_raw_buffer_store_b32(tag, wsr, WS_FK + (bj * 16 + bi) * 128, 0, 16);
    {
      const f32x4 cB = *(LAS f32x4*)(sm + L_CS + 5 * 64 + g * 16) + *(LAS f32x4*)(sm + L_CS + 512 + 5 * 64 + g * 16), cC = *(LAS f32x4*)(sm + L_CS + 6 * 64 + g * 16) + *(LAS f32x4*)(sm + L_CS + 512 + 6 * 64 + g * 16),
                  cA = *(LAS f32x4*)(sm + L_CS + 7 * 64 + g * 16) + *(LAS f32x4*)(sm + L_CS + 512 + 7 * 64 + g * 16);
      u32x4 b0[2]; u32x2 qa[2];
#pragma unroll
      for (int t = 0; t < 2; t++) {
        const f32x4 vB = acc[t][5] - cB * mean[t], vC = acc[t][6] - cC * mean[t], vA = acc[t][7] - cA * mean[t];
        b0[t] = (u32x4){pk2(vB[0], vB[1]), pk2(vB[2], vB[3]), pk2(vC[0], vC[1]), pk2(vC[2], vC[3])};
        qa[t] = (u32x2){pk2(vA[0], vA[1]), pk2(vA[2], vA[3])};
      }
      *(LAS u32x4*)(sm + L_HO + v * 4096 + l * 16) = b0[1]; *(LAS u32x2*)(sm + L_HO + v * 4096 + 1024 + l * 8) = qa[1];
      cnt_signal(12 + v);
      phiq_tile(b0[0], qa[0], rs0);
    }
    {
      const int ch = v * 53 + l;
      const bool val = l < 53 && ch < 210;
      const u32 base = val ? WS_PART + (qj * 16) * PSTR + ch * 16 : OOB;
      const int row = ch / 5, cq = ch % 5;
      u32 ka = L_KVR + row * 96 + cq * 16 + (cq > 2 ? 8 : 0), kb = L_KVR + row * 96 + cq * 16 + 8 + (cq >= 2 ? 8 : 0);
      PIN(ka); PIN(kb);
      if (v == 0) { poll16(wsr, WS_FK + qj * 2048, tag, l, tmo); cnt_signal(1); } else cnt_wait(sm, 1, 3);
      u32x4 pv[16]; u32 spins = 0;
      f32x2 s[4];
#pragma unroll
      for (int e = 0; e < 4; e++) s[e] = (f32x2){0.f, 0.f};
#pragma unroll
      for (int p = 0; p < 16; p++) pv[p] = __builtin_amdgcn_raw_buffer_load_b128(wsr, base, p * PSTR, 16);
      u32 todo = 0, mxv = 0;
#pragma unroll
      for (int p = 0; p < 16; p++) {
        mxv = mx4(mxv, pv[p]);
#pragma unroll
        for (int e = 0; e < 4; e++) s[e] += (f32x2){bflo(pv[p][e]), bfhi(pv[p][e])};
      }
      if (anysent(mxv)) {
        todo = 0xFFFFu;
#pragma unroll
        for (int e = 0; e < 4; e++) s[e] = (f32x2){0.f, 0.f};
      }
      while (todo) {
        if (++spins > SPIN_CAP) { tmo = true; break; }
        wait_until(__builtin_amdgcn_s_memrealtime() + D_BK);
        CL<16>::run(pv, todo, base, wsq, PSTR);
        asm volatile("s_waitcnt vmcnt(0)" : "+v"(pv[0]), "+v"(pv[1]), "+v"(pv[2]), "+v"(pv[3]), "+v"(pv[4]), "+v"(pv[5]), "+v"(pv[6]), "+v"(pv[7]),
                     "+v"(pv[8]), "+v"(pv[9]), "+v"(pv[10]), "+v"(pv[11]), "+v"(pv[12]), "+v"(pv[13]), "+v"(pv[14]), "+v"(pv[15]) :: "memory");
        const u32 m = todo;
#pragma unroll
        for (int p = 0; p < 16; p++) {
          if (m & (1u << p)) {
            if (!anysent(mx4(0u, pv[p]))) {
              todo &= ~(1u << p);
#pragma unroll
              for (int e = 0; e < 4; e++) s[e] += (f32x2){bflo(pv[p][e]), bfhi(pv[p][e])};
            }
          }
        }
      }
      if (val) {
        *(LAS u32x2*)(sm + ka) = (u32x2){pk2(s[0][0], s[0][1]), pk2(s[1][0], s[1][1])};
        *(LAS u32x2*)(sm + kb) = (u32x2){pk2(s[2][0], s[2][1]), pk2(s[3][0], s[3][1])};
      }
    }
    BARRIER_LDS();
    o_stage();
    {
      const int tok = bi * 128 + th * 64 + v * 16 + c16;
      u32x2 xr[5];
      const u32x2* xp = (const u32x2*)(x + tok * 640 + co * 80 + 20 * g);
#pragma unroll
      for (int e = 0; e < 5; e++) xr[e] = xp[e];
      const f32x2 st = *(LAS f32x2*)(sm + L_STAT + (th * 64 + v * 16 + c16) * 8);
      const float a1 = 1.f + st[1], a0 = -st[0] * st[1];
      f32x4 base4[5];
#pragma unroll
      for (int T = 0; T < 5; T++) { base4[T] = (f32x4){bflo(xr[T].x) * a1 + a0, bfhi(xr[T].x) * a1 + a0, bflo(xr[T].y) * a1 + a0, bfhi(xr[T].y) * a1 + a0}; PIN(base4[T]); }
      const u32 ob = WS_O + ((bi * 8 + 4 * th + v) * 80) * 256 + l * 16;
      u32x4 of[2][10]; u32 spins = 0;
      f32x4 oc[5];
      LAS char* wr2 = kvrd + 51200;
      const u32 ridx = __builtin_amdgcn_readfirstlane(*(LAS u32*)(sm + L_Y + 320));
      cnt_wait(sm, 2, 2);
      if (v == 0) { poll16(wsr, WS_FO + bi * 2048, tag, l, tmo); cnt_signal(1); } else cnt_wait(sm, 1, 4);
#pragma unroll
      for (int n = 0; n < 10; n++) of[0][n] = __builtin_amdgcn_raw_buffer_load_b128(wsr, ob, n * 1024, 16);
#pragma unroll
      for (int n = 0; n < 10; n++) of[1][n] = __builtin_amdgcn_raw_buffer_load_b128(wsr, ob + 10240, n * 1024, 16);
      for (;;) {
        u32x4 wf[4][5];
#pragma unroll
        for (int n = 0; n < 3; n++)
#pragma unroll
          for (int T = 0; T < 5; T++) wf[n][T] = trd2(kvrd + n * 5120 + 8 * T, 2560);
#pragma unroll
        for (int T = 0; T < 5; T++) oc[T] = base4[T];
        u32 m = 0;
        SB();
#pragma unroll
        for (int n = 0; n < 20; n++) {
          if (n + 3 < 20) {
#pragma unroll
            for (int T = 0; T < 5; T++) wf[(n + 3) & 3][T] = trd2((n + 3 < 10 ? kvrd + (n + 3) * 5120 : wr2 + (n - 7) * 5120) + 8 * T, 2560);
          }
#pragma unroll
          for (int T = 0; T < 5; T++) oc[T] = mfma(wf[n & 3][T], of[n / 10][n % 10], oc[T]);
          m = mx4(m, of[n / 10][n % 10]);
#pragma unroll
          for (int i = 0; i < 5; i++) { SGB(0x8, 1); SGB(0x100, 2); }
          SB();
        }
        if (!anysent(m)) break;
        u32 todo = 0;
#pragma unroll
        for (int n = 0; n < 10; n++) todo |= anysent(mx4(mx4(0u, of[0][n]), of[1][n])) ? (1u << n) : 0u;
        while (todo) {
          if (++spins > SPIN_CAP) { tmo = true; break; }
          CL<10>::run(of[0], todo, ob, wsq, 1024);
          CL<10>::run(of[1], todo, ob + 10240, wsq, 1024);
          asm volatile("s_waitcnt vmcnt(0)" : "+v"(of[0][0]), "+v"(of[0][1]), "+v"(of[0][2]), "+v"(of[0][3]), "+v"(of[0][4]), "+v"(of[0][5]), "+v"(of[0][6]), "+v"(of[0][7]), "+v"(of[0][8]), "+v"(of[0][9]),
                       "+v"(of[1][0]), "+v"(of[1][1]), "+v"(of[1][2]), "+v"(of[1][3]), "+v"(of[1][4]), "+v"(of[1][5]), "+v"(of[1][6]), "+v"(of[1][7]), "+v"(of[1][8]), "+v"(of[1][9]) :: "memory");
          const u32 mm = todo;
#pragma unroll
          for (int n = 0; n < 10; n++) if (mm & (1u << n)) { if (!anysent(mx4(mx4(0u, of[0][n]), of[1][n]))) todo &= ~(1u << n); }
          if (todo) wait_until(__builtin_amdgcn_s_memrealtime() + D_BK);
        }
        if (tmo) break;
      }
      if ((int)(ridx >> 4) == (tok >> 4)) {
        cnt_wait(sm, 3, 1);
        if (ridx == (u32)tok) {
#pragma unroll
          for (int T = 0; T < 5; T++) oc[T] += *(LAS f32x4*)(sm + L_Y + (20 * g + 4 * T) * 4);
        }
      }
      const __amdgpu_buffer_rsrc_t outr = __builtin_amdgcn_make_buffer_rsrc(out, 0, 2048 * 1280, 0x00020000);
      const u32 oo = tok * 1280 + co * 160 + 40 * g;
      __builtin_amdgcn_raw_buffer_store_b128((u32x4){pk2(oc[0][0], oc[0][1]), pk2(oc[0][2], oc[0][3]), pk2(oc[1][0], oc[1][1]), pk2(oc[1][2], oc[1][3])}, outr, oo, 0, 0);
      __builtin_amdgcn_raw_buffer_store_b128((u32x4){pk2(oc[2][0], oc[2][1]), pk2(oc[2][2], oc[2][3]), pk2(oc[3][0], oc[3][1]), pk2(oc[3][2], oc[3][3])}, outr, oo + 16, 0, 0);
      __builtin_amdgcn_raw_buffer_store_b64((u32x2){pk2(oc[4][0], oc[4][1]), pk2(oc[4][2], oc[4][3])}, outr, oo + 32, 0, 0);
    }
  }
  if (tmo && l == 0) *(u32*)(ws + WS_TMO) = 1u;
}
}

extern "C" void kernel_launch(void* const* d_in, const int* in_sizes, int n_in, void* d_out, int out_size, void* d_ws, size_t ws_size, hipStream_t stream) {
  using namespace fk;
  (void)hipFuncSetAttribute((const void*)fused, hipFuncAttributeMaxDynamicSharedMemorySize, L_TOTAL);
  fused<<<256, 512, L_TOTAL, stream>>>((const u16*)d_in[0], (const u16*)d_in[1], (const u16*)d_in[2], (const u16*)d_in[3], (const u16*)d_in[5],
                                       (const u16*)d_in[6], (const u16*)d_in[7], (const u16*)d_in[8], (u16*)d_out, (char*)d_ws);
}
```
